```python
import torch

def _softmax(x, axis=-1):
    return torch.softmax(x, dim=axis)
def _gelu(x):
    return torch.nn.functional.gelu(x, approximate="tanh")
def _silu(x):
    return torch.nn.functional.silu(x)
def _sigmoid(x):
    return torch.sigmoid(x)
def _layernorm(x, axis=-1, eps=1e-5):
    m = x.mean(dim=axis, keepdim=True)
    v = x.var(dim=axis, keepdim=True, unbiased=False)
    return (x - m) / torch.sqrt(v + eps)
def _scatter_add(tgt, idx, src, axis=0):
    return tgt.clone().index_add_(axis, idx, src)
def _segment_sum(src, seg, n):
    out = torch.zeros((n,) + src.shape[1:], dtype=src.dtype)
    return out.index_add_(0, seg, src)
def _pad(x, pw):
    flat = tuple(v for pair in reversed(pw) for v in pair)
    return torch.nn.functional.pad(x, flat)

kernel_name = 'recurrent_scan__gqa_block_cached__swiglu_ffn__decode'

def setup_inputs():

    _w = (lambda a: (lambda t: t.to(torch.bfloat16) if t.is_floating_point() else t)(torch.as_tensor(a)))
    def _A_setup():
        B, Sq, D = 1, 1, 576
        Hq, Hkv, Sk, Dh = 32, 16, 65536, 128
        assert Hq % Hkv == 0, (Hq, Hkv)
        return {
            "x": _w((lambda *s: torch.randn(*s, dtype=torch.bfloat16) * 0.05)(B, Sq, D)),
            "k_cache": _w((lambda *s: torch.randn(*s, dtype=torch.bfloat16) * 1.6)(B, Hkv, Sk, Dh)),
            "v_cache": _w((lambda *s: torch.randn(*s, dtype=torch.bfloat16) * 0.2)(B, Hkv, Sk, Dh)),
            "wq": _w((lambda *s: torch.randn(*s, dtype=torch.bfloat16) * 1.6)(D, Hq * Dh) * (D ** -0.5)),
            "wo": _w((lambda *s: torch.randn(*s, dtype=torch.bfloat16) * 0.2)(Hq * Dh, D) * ((Hq * Dh) ** -0.5)),
        }
    _A_in = _A_setup()

    def _B_setup():
        B, S, D, F = 1, 1, 576, 4608
        return {
            "x": _w((lambda *s: torch.randn(*s, dtype=torch.bfloat16) * 0.05)(B, S, D)),
            "w_gate": _w((lambda *s: torch.randn(*s, dtype=torch.bfloat16) * 0.1)(D, F)),
            "w_up": _w((lambda *s: torch.randn(*s, dtype=torch.bfloat16) * 0.1)(D, F)),
            "w_down": _w((lambda *s: torch.randn(*s, dtype=torch.bfloat16) * 0.1)(F, D)),
        }
    _B_in = _B_setup()

    return {
        "A_x": _A_in["x"],
        "A_k_cache": _A_in["k_cache"],
        "A_v_cache": _A_in["v_cache"],
        "A_wq": _A_in["wq"],
        "A_wo": _A_in["wo"],
        "B_x": _B_in["x"],
        "B_w_gate": _B_in["w_gate"],
        "B_w_up": _B_in["w_up"],
        "B_w_down": _B_in["w_down"],
    }

def reference(A_x, A_k_cache, A_v_cache, A_wq, A_wo, B_x, B_w_gate, B_w_up, B_w_down):
    def _A_ref(x, k_cache, v_cache, wq, wo):
        B, Sq, D = x.shape
        Hq, Dh = 32, 128
        Hkv = k_cache.shape[1]
        rep = Hq // Hkv
        q = (lambda x, *d: x.permute(*d))((x @ wq).reshape(B, Sq, Hq, Dh), 0, 2, 1, 3)
        k = (lambda x, n, axis: torch.repeat_interleave(x, n, dim=axis))(k_cache, rep, axis=1)
        v = (lambda x, n, axis: torch.repeat_interleave(x, n, dim=axis))(v_cache, rep, axis=1)
        scores = torch.einsum("bhqd,bhkd->bhqk", q, k) * (Dh ** -0.5)
        p = _softmax(scores, axis=-1)
        o = torch.einsum("bhqk,bhkd->bhqd", p, v)
        return (lambda x, *d: x.permute(*d))(o, 0, 2, 1, 3).reshape(B, Sq, Hq * Dh) @ wo

    def _B_ref(x, w_gate, w_up, w_down):
        g = _silu(x @ w_gate)
        u = x @ w_up
        return (g * u) @ w_down

    x = A_x
    xn = _layernorm(x, axis=-1)
    s = _A_ref(xn, A_k_cache, A_v_cache, A_wq, A_wo)
    g = _silu(_B_ref(xn, B_w_gate, B_w_up, B_w_down))
    return x + s * g
```

```cpp
#include <hip/hip_runtime.h>
#include <hip/hip_bf16.h>
#include <cstdint>

typedef unsigned u32x4 __attribute__((ext_vector_type(4)));
typedef float f32x4 __attribute__((ext_vector_type(4)));
typedef short bf16x8 __attribute__((ext_vector_type(8)));
typedef __bf16 bf16x2 __attribute__((ext_vector_type(2)));

constexpr int D = 576, HQ = 32, HKV = 16, SK = 65536, DH = 128, F = 4608;
constexpr int NSPLIT = 16;

__device__ __forceinline__ float bf2f(unsigned short b) { return __builtin_bit_cast(float, (unsigned)b << 16); }
__device__ __forceinline__ float bflo(unsigned w) { return __builtin_bit_cast(float, w << 16); }
__device__ __forceinline__ float bfhi(unsigned w) { return __builtin_bit_cast(float, w & 0xffff0000u); }
__device__ __forceinline__ unsigned cvtpk(float lo, float hi) {
  unsigned r; asm("v_cvt_pk_bf16_f32 %0, %1, %2" : "=v"(r) : "v"(lo), "v"(hi)); return r;
}
__device__ __forceinline__ float rbf(float x) { return bflo(cvtpk(x, x)); }
__device__ __forceinline__ float dot2(unsigned k, unsigned q, float acc) {
  return __builtin_amdgcn_fdot2_f32_bf16(__builtin_bit_cast(bf16x2, k), __builtin_bit_cast(bf16x2, q), acc, false);
}
template <int CTRL> __device__ __forceinline__ float dpp(float x) {
  return __builtin_bit_cast(float, __builtin_amdgcn_mov_dpp(__builtin_bit_cast(int, x), CTRL, 0xf, 0xf, true));
}
template <int CTRL> __device__ __forceinline__ unsigned dppu(unsigned x) {
  return (unsigned)__builtin_amdgcn_mov_dpp((int)x, CTRL, 0xf, 0xf, true);
}
constexpr int XOR1 = 0xB1, XOR2 = 0x4E, XOR7 = 0x141, XOR8 = 0x128;
constexpr int ROR(int n) { return 0x120 + n; }
__device__ __forceinline__ float xrow_max(float x) {
  auto s = __builtin_amdgcn_permlane16_swap(__float_as_uint(x), __float_as_uint(x), false, false);
  x = fmaxf(__uint_as_float(s[0]), __uint_as_float(s[1]));
  auto t = __builtin_amdgcn_permlane32_swap(__float_as_uint(x), __float_as_uint(x), false, false);
  return fmaxf(__uint_as_float(t[0]), __uint_as_float(t[1]));
}
__device__ __forceinline__ float xrow_sum(float x) {
  auto s = __builtin_amdgcn_permlane16_swap(__float_as_uint(x), __float_as_uint(x), false, false);
  x = __uint_as_float(s[0]) + __uint_as_float(s[1]);
  auto t = __builtin_amdgcn_permlane32_swap(__float_as_uint(x), __float_as_uint(x), false, false);
  return __uint_as_float(t[0]) + __uint_as_float(t[1]);
}
__device__ __forceinline__ float wave_sum(float x) {
  x += dpp<XOR1>(x); x += dpp<XOR2>(x); x += dpp<XOR7>(x); x += dpp<XOR8>(x);
  return xrow_sum(x);
}
__device__ __forceinline__ u32x4 ldnt(const void* p) { return __builtin_nontemporal_load(reinterpret_cast<const u32x4*>(p)); }

constexpr size_t WS_Q = 0;
constexpr size_t WS_Y = 16384;
constexpr size_t WS_PML = WS_Y + 144 * 576 * 4;
constexpr size_t WS_PO = WS_PML + 32 * 16 * 2 * 4;
constexpr size_t WS_S = WS_PO + 32 * 16 * 128 * 4;

__device__ __forceinline__ void layernorm_to_lds(const unsigned short* x, float* xn, int tid, int nthr) {
  const int lane = tid & 63;
  float v[9]; float s = 0.f;
#pragma unroll
  for (int i = 0; i < 9; ++i) { v[i] = bf2f(x[lane + 64 * i]); s += v[i]; }
  s = wave_sum(s);
  const float mean = s * (1.f / D);
  float ss = 0.f;
#pragma unroll
  for (int i = 0; i < 9; ++i) { float d = v[i] - mean; ss += d * d; }
  ss = wave_sum(ss);
  const float m_b = rbf(mean), v_b = rbf(ss * (1.f / D));
  const float den = rbf(sqrtf(rbf(v_b + 1e-5f)));
  if (tid < 64) {
#pragma unroll
    for (int i = 0; i < 9; ++i) xn[lane + 64 * i] = rbf(rbf(v[i] - m_b) / den);
  }
  __syncthreads();
}

__device__ __forceinline__ float silu_f(float x) { return x / (1.f + __expf(-x)); }

__global__ void __launch_bounds__(576) k_pre(const unsigned short* __restrict__ x, const unsigned short* __restrict__ wq,
                                             const unsigned short* __restrict__ wg, const unsigned short* __restrict__ wu,
                                             const unsigned short* __restrict__ wd, unsigned char* ws) {
  __shared__ float xn[D];
  __shared__ float red[576 * 8];
  __shared__ float hs[64];
  const int tid = threadIdx.x;
  if (blockIdx.x < 64) {
    const int cb = blockIdx.x, ch = tid & 7, rl = tid >> 3;
    u32x4 w[8];
#pragma unroll
    for (int i = 0; i < 8; ++i) w[i] = ldnt(wq + (size_t)(rl + 72 * i) * 4096 + cb * 64 + ch * 8);
    layernorm_to_lds(x, xn, tid, 576);
    float a[8] = {0, 0, 0, 0, 0, 0, 0, 0};
#pragma unroll
    for (int i = 0; i < 8; ++i) { const float xv = xn[rl + 72 * i];
#pragma unroll
      for (int k = 0; k < 4; ++k) { a[2 * k] = fmaf(xv, bflo(w[i][k]), a[2 * k]); a[2 * k + 1] = fmaf(xv, bfhi(w[i][k]), a[2 * k + 1]); } }
#pragma unroll
    for (int k = 0; k < 8; ++k) red[rl * 64 + ch * 8 + k] = a[k];
    __syncthreads();
    if (tid < 64) {
      float s = 0.f;
      for (int r = 0; r < 72; ++r) s += red[r * 64 + tid];
      reinterpret_cast<unsigned short*>(ws + WS_Q)[cb * 64 + tid] = (unsigned short)(cvtpk(s, s) & 0xffffu);
    }
  } else {
    const int u = blockIdx.x - 64, ch = tid & 3, rl = tid >> 2;
    u32x4 g[4], up[4], dn[4];
#pragma unroll
    for (int i = 0; i < 4; ++i) { g[i] = ldnt(wg + (size_t)(rl + 144 * i) * F + u * 32 + ch * 8); up[i] = ldnt(wu + (size_t)(rl + 144 * i) * F + u * 32 + ch * 8); }
    const int dc = tid % 72, drg = tid / 72;
#pragma unroll
    for (int i = 0; i < 4; ++i) dn[i] = ldnt(wd + (size_t)(u * 32 + drg * 4 + i) * D + dc * 8);
    layernorm_to_lds(x, xn, tid, 576);
    float a[8] = {0, 0, 0, 0, 0, 0, 0, 0}, b[8] = {0, 0, 0, 0, 0, 0, 0, 0};
#pragma unroll
    for (int i = 0; i < 4; ++i) { const float xv = xn[rl + 144 * i];
#pragma unroll
      for (int k = 0; k < 4; ++k) {
        a[2 * k] = fmaf(xv, bflo(g[i][k]), a[2 * k]); a[2 * k + 1] = fmaf(xv, bfhi(g[i][k]), a[2 * k + 1]);
        b[2 * k] = fmaf(xv, bflo(up[i][k]), b[2 * k]); b[2 * k + 1] = fmaf(xv, bfhi(up[i][k]), b[2 * k + 1]); } }
#pragma unroll
    for (int k = 0; k < 8; ++k) {
      a[k] += dpp<ROR(4)>(a[k]); a[k] += dpp<ROR(8)>(a[k]); a[k] = xrow_sum(a[k]);
      b[k] += dpp<ROR(4)>(b[k]); b[k] += dpp<ROR(8)>(b[k]); b[k] = xrow_sum(b[k]);
    }
    const int wave = tid >> 6, lane = tid & 63;
    if (lane < 4) {
#pragma unroll
      for (int k = 0; k < 8; ++k) { red[wave * 64 + ch * 8 + k] = a[k]; red[wave * 64 + 32 + ch * 8 + k] = b[k]; }
    }
    __syncthreads();
    if (tid < 32) {
      float sg = 0.f, su = 0.f;
      for (int w = 0; w < 9; ++w) { sg += red[w * 64 + tid]; su += red[w * 64 + 32 + tid]; }
      const float gb = rbf(silu_f(rbf(sg)));
      hs[tid] = rbf(gb * rbf(su));
    }
    __syncthreads();
    float c[8] = {0, 0, 0, 0, 0, 0, 0, 0};
#pragma unroll
    for (int i = 0; i < 4; ++i) { const float hv = hs[drg * 4 + i];
#pragma unroll
      for (int k = 0; k < 4; ++k) { c[2 * k] = fmaf(hv, bflo(dn[i][k]), c[2 * k]); c[2 * k + 1] = fmaf(hv, bfhi(dn[i][k]), c[2 * k + 1]); } }
#pragma unroll
    for (int k = 0; k < 8; ++k) red[drg * 576 + dc * 8 + k] = c[k];
    __syncthreads();
    float s = 0.f;
#pragma unroll
    for (int r = 0; r < 8; ++r) s += red[r * 576 + tid];
    reinterpret_cast<float*>(ws + WS_Y)[u * 576 + tid] = s;
  }
}

#define NWAVES 4
constexpr int NW = NWAVES, NTHR = NW * 64;
constexpr int KEYS_PER_WG = SK / NSPLIT;
constexpr int TILE = 32;
constexpr int ITERS = KEYS_PER_WG / (TILE * NW);

__global__ void __launch_bounds__(NTHR, 2) k_attn(const unsigned short* __restrict__ kc, const unsigned short* __restrict__ vc, unsigned char* ws) {
  __shared__ float mrg[NW][2][4 + DH];
  const int tid = threadIdx.x, lane = tid & 63, wave = __builtin_amdgcn_readfirstlane(tid >> 6);
  const int r = lane >> 4, c = lane & 15;
  const int kvh = blockIdx.x >> 4, split = blockIdx.x & 15;
  const size_t base = ((size_t)kvh * SK + (size_t)split * KEYS_PER_WG + (size_t)wave * TILE) * DH + (size_t)lane * 8;
  const unsigned short* kp = kc + base;
  const unsigned short* vp = vc + base;
  u32x4 K[8], V[8];
  const u32x4 q0 = *reinterpret_cast<const u32x4*>(ws + WS_Q + ((size_t)(kvh * 2) * DH + c * 8) * 2);
  const u32x4 q1 = *reinterpret_cast<const u32x4*>(ws + WS_Q + ((size_t)(kvh * 2 + 1) * DH + c * 8) * 2);
  __builtin_amdgcn_sched_barrier(0);
#pragma unroll
  for (int i = 0; i < 8; ++i) K[i] = ldnt(kp + i * 512);
#pragma unroll
  for (int i = 0; i < 8; ++i) V[i] = ldnt(vp + i * 512);
  __builtin_amdgcn_sched_barrier(0);
  constexpr float C2 = 0.088388347648318440f * 1.4426950408889634f;
  float m = -INFINITY, l = 0.f;
  f32x4 acc[8];
#pragma unroll
  for (int e = 0; e < 8; ++e) acc[e] = f32x4{0.f, 0.f, 0.f, 0.f};

  for (int it = 0; it < ITERS; ++it) {
    kp += NW * TILE * DH; vp += NW * TILE * DH;
    const bool more = it + 1 < ITERS;
    float x[16];
#pragma unroll
    for (int i = 0; i < 8; ++i) {
      float d0 = 0.f, d1 = 0.f;
#pragma unroll
      for (int k = 0; k < 4; ++k) { d0 = dot2(K[i][k], q0[k], d0); d1 = dot2(K[i][k], q1[k], d1); }
      x[2 * i] = d0; x[2 * i + 1] = d1;
      if (more) K[i] = ldnt(kp + i * 512);
    }
#define TR_STEP(HALF, CTRL, BIT) _Pragma("unroll") for (int h = 0; h < (HALF); ++h) { \
      const float lo_ = x[h], hi_ = x[h + (HALF)]; const float t1_ = lo_ + dpp<CTRL>(lo_), t2_ = hi_ + dpp<CTRL>(hi_); \
      x[h] = (c & (BIT)) ? t2_ : t1_; }
    TR_STEP(8, XOR8, 8) TR_STEP(4, XOR7, 4) TR_STEP(2, XOR2, 2) TR_STEP(1, XOR1, 1)
#undef TR_STEP
    const float s = x[0] * C2;
    if (__any(s > m)) {
      float pm = s; pm = fmaxf(pm, dpp<ROR(2)>(pm)); pm = fmaxf(pm, dpp<ROR(4)>(pm)); pm = fmaxf(pm, dpp<ROR(8)>(pm)); pm = xrow_max(pm);
      const float mn = fmaxf(m, pm);
      const float alpha = __builtin_amdgcn_exp2f(m - mn);
      m = mn; l *= alpha;
#pragma unroll
      for (int e = 0; e < 8; ++e) acc[e] *= alpha;
    }
    const float p = __builtin_amdgcn_exp2f(s - m);
    l += p;
    const unsigned w1 = cvtpk(p, dpp<ROR(14)>(p));
    u32x4 pb;
    pb[0] = w1; pb[1] = dppu<ROR(12)>(w1); pb[2] = dppu<ROR(8)>(w1); pb[3] = dppu<ROR(4)>(w1);
#pragma unroll
    for (int k = 0; k < 4; ++k) {
      u32x4 alo, ahi;
#pragma unroll
      for (int j = 0; j < 4; ++j) {
        alo[j] = __builtin_amdgcn_perm(V[2 * j + 1][k], V[2 * j][k], 0x05040100u);
        ahi[j] = __builtin_amdgcn_perm(V[2 * j + 1][k], V[2 * j][k], 0x07060302u);
      }
      acc[2 * k] = __builtin_amdgcn_mfma_f32_16x16x32_bf16(__builtin_bit_cast(bf16x8, alo), __builtin_bit_cast(bf16x8, pb), acc[2 * k], 0, 0, 0);
      acc[2 * k + 1] = __builtin_amdgcn_mfma_f32_16x16x32_bf16(__builtin_bit_cast(bf16x8, ahi), __builtin_bit_cast(bf16x8, pb), acc[2 * k + 1], 0, 0, 0);
    }
    if (more) {
#pragma unroll
      for (int i = 0; i < 8; ++i) V[i] = ldnt(vp + i * 512);
    }
  }
  l += dpp<ROR(2)>(l); l += dpp<ROR(4)>(l); l += dpp<ROR(8)>(l); l = xrow_sum(l);
  if (c < 2) {
#pragma unroll
    for (int e = 0; e < 8; ++e)
#pragma unroll
      for (int i = 0; i < 4; ++i) mrg[wave][c][4 + 8 * (4 * r + i) + e] = acc[e][i];
    if (r == 0) { mrg[wave][c][0] = m; mrg[wave][c][1] = l; }
  }
  __syncthreads();
  if (tid < 256) {
    const int g = tid >> 7, d = tid & 127;
    float M = -INFINITY;
#pragma unroll
    for (int w = 0; w < NW; ++w) M = fmaxf(M, mrg[w][g][0]);
    float L = 0.f, o = 0.f;
#pragma unroll
    for (int w = 0; w < NW; ++w) { const float wt = __builtin_amdgcn_exp2f(mrg[w][g][0] - M); L = fmaf(wt, mrg[w][g][1], L); o = fmaf(wt, mrg[w][g][4 + d], o); }
    const int slot = (kvh * 2 + g) * NSPLIT + split;
    reinterpret_cast<float*>(ws + WS_PO)[(size_t)slot * DH + d] = o;
    if (d == 0) { reinterpret_cast<float*>(ws + WS_PML)[slot * 2] = M; reinterpret_cast<float*>(ws + WS_PML)[slot * 2 + 1] = L; }
  }
}

__global__ void __launch_bounds__(576) k_comb(const unsigned short* __restrict__ wo, unsigned char* ws) {
  __shared__ float os[DH];
  __shared__ float red[8 * 576];
  const int tid = threadIdx.x, h = blockIdx.x;
  const int dc = tid % 72, rg = tid / 72;
  u32x4 w[16];
#pragma unroll
  for (int i = 0; i < 16; ++i) w[i] = ldnt(wo + (size_t)(h * DH + rg * 16 + i) * D + dc * 8);
  if (tid < DH) {
    const float* pml = reinterpret_cast<const float*>(ws + WS_PML) + h * NSPLIT * 2;
    const float* po = reinterpret_cast<const float*>(ws + WS_PO) + (size_t)h * NSPLIT * DH;
    float M = -INFINITY;
    for (int s = 0; s < NSPLIT; ++s) M = fmaxf(M, pml[2 * s]);
    float L = 0.f, o = 0.f;
    for (int s = 0; s < NSPLIT; ++s) { const float wt = __builtin_amdgcn_exp2f(pml[2 * s] - M); L = fmaf(wt, pml[2 * s + 1], L); o = fmaf(wt, po[s * DH + tid], o); }
    os[tid] = rbf(o / L);
  }
  __syncthreads();
  float a[8] = {0, 0, 0, 0, 0, 0, 0, 0};
#pragma unroll
  for (int i = 0; i < 16; ++i) { const float ov = os[rg * 16 + i];
#pragma unroll
    for (int k = 0; k < 4; ++k) { a[2 * k] = fmaf(ov, bflo(w[i][k]), a[2 * k]); a[2 * k + 1] = fmaf(ov, bfhi(w[i][k]), a[2 * k + 1]); } }
#pragma unroll
  for (int k = 0; k < 8; ++k) red[rg * 576 + dc * 8 + k] = a[k];
  __syncthreads();
  float s = 0.f;
#pragma unroll
  for (int r = 0; r < 8; ++r) s += red[r * 576 + tid];
  reinterpret_cast<float*>(ws + WS_S)[h * 576 + tid] = s;
}

__global__ void __launch_bounds__(256) k_final(const unsigned short* __restrict__ x, unsigned char* ws, unsigned short* out) {
  __shared__ float red[2][4][64];
  const int tid = threadIdx.x, col = blockIdx.x * 64 + (tid & 63), part = tid >> 6;
  const float* S = reinterpret_cast<const float*>(ws + WS_S);
  const float* Y = reinterpret_cast<const float*>(ws + WS_Y);
  float s = 0.f, y = 0.f;
#pragma unroll
  for (int i = 0; i < 8; ++i) s += S[(part * 8 + i) * 576 + col];
#pragma unroll
  for (int i = 0; i < 36; ++i) y += Y[(part * 36 + i) * 576 + col];
  red[0][part][tid & 63] = s; red[1][part][tid & 63] = y;
  __syncthreads();
  if (tid < 64) {
    s = red[0][0][tid] + red[0][1][tid] + red[0][2][tid] + red[0][3][tid];
    y = red[1][0][tid] + red[1][1][tid] + red[1][2][tid] + red[1][3][tid];
    const float sb = rbf(s), g = rbf(silu_f(rbf(y)));
    const float o = bf2f(x[col]) + rbf(sb * g);
    out[col] = (unsigned short)(cvtpk(o, o) & 0xffffu);
  }
}

extern "C" void kernel_launch(void* const* d_in, const int* in_sizes, int n_in, void* d_out, int out_size, void* d_ws, size_t ws_size,
                              hipStream_t stream) {
  const unsigned short* x = (const unsigned short*)d_in[0];
  const unsigned short* kc = (const unsigned short*)d_in[1];
  const unsigned short* vc = (const unsigned short*)d_in[2];
  const unsigned short* wq = (const unsigned short*)d_in[3];
  const unsigned short* wo = (const unsigned short*)d_in[4];
  const unsigned short* wg = (const unsigned short*)d_in[6];
  const unsigned short* wu = (const unsigned short*)d_in[7];
  const unsigned short* wd = (const unsigned short*)d_in[8];
  unsigned char* ws = (unsigned char*)d_ws;
  k_pre<<<64 + 144, 576, 0, stream>>>(x, wq, wg, wu, wd, ws);
  k_attn<<<HKV * NSPLIT, NTHR, 0, stream>>>(kc, vc, ws);
  k_comb<<<32, 576, 0, stream>>>(wo, ws);
  k_final<<<9, 256, 0, stream>>>(x, ws, (unsigned short*)d_out);
}
```

```cpp
#include <hip/hip_runtime.h>
#include <hip/hip_bf16.h>
#include <cstdint>

typedef unsigned u32x4 __attribute__((ext_vector_type(4)));
typedef unsigned u32x2 __attribute__((ext_vector_type(2)));
typedef float f32x4 __attribute__((ext_vector_type(4)));
typedef short bf16x8 __attribute__((ext_vector_type(8)));
typedef short bf16x4 __attribute__((ext_vector_type(4)));
typedef __bf16 bf16x2 __attribute__((ext_vector_type(2)));
typedef unsigned long long u64;
#define GAS __attribute__((address_space(1)))
#define LAS __attribute__((address_space(3)))

constexpr int D = 576, SK = 65536, DH = 128, F = 4608;
#define NSTATIC 52
#define NPRE 4
#define FRING 18
constexpr int TILE = 16;
constexpr int TPC = 4096 / TILE;
constexpr int POOL_N = 16 * (TPC - 4 * NSTATIC);
constexpr unsigned SPIN_CAP = 1u << 16;

constexpr size_t WS_EPOCH = 0;
constexpr size_t WS_CTR = 4096;
constexpr size_t WS_QG = 16384;
constexpr size_t WS_YG = WS_QG + 64 * 4 * 64 * 8;
constexpr size_t WS_PG = WS_YG + 72 * 144 * 8 * 8;
constexpr size_t WS_SG = WS_PG + 16 * 8 * 16 * 34 * 8;
constexpr size_t WS_END = WS_SG + 72 * 128 * 8 * 8;
#define STAMP(i) do {} while (0)

__device__ __forceinline__ float bf2f(unsigned short b) { return __builtin_bit_cast(float, (unsigned)b << 16); }
__device__ __forceinline__ float bflo(unsigned w) { return __builtin_bit_cast(float, w << 16); }
__device__ __forceinline__ float bfhi(unsigned w) { return __builtin_bit_cast(float, w & 0xffff0000u); }
__device__ __forceinline__ unsigned cvtpk(float lo, float hi) {
  unsigned r; asm("v_cvt_pk_bf16_f32 %0, %1, %2" : "=v"(r) : "v"(lo), "v"(hi)); return r;
}
__device__ __forceinline__ float rbf(float x) { return bflo(cvtpk(x, x)); }
__device__ __forceinline__ float dot2(unsigned k, unsigned q, float acc) {
  return __builtin_amdgcn_fdot2_f32_bf16(__builtin_bit_cast(bf16x2, k), __builtin_bit_cast(bf16x2, q), acc, false);
}
template <int CTRL> __device__ __forceinline__ float dpp(float x) {
  return __builtin_bit_cast(float, __builtin_amdgcn_mov_dpp(__builtin_bit_cast(int, x), CTRL, 0xf, 0xf, true));
}
template <int CTRL> __device__ __forceinline__ unsigned dppu(unsigned x) {
  return (unsigned)__builtin_amdgcn_mov_dpp((int)x, CTRL, 0xf, 0xf, true);
}
constexpr int XOR1 = 0xB1, XOR2 = 0x4E, XOR7 = 0x141, XOR8 = 0x128;
constexpr int ROR(int n) { return 0x120 + n; }
__device__ __forceinline__ float xrow_max(float x) {
  auto s = __builtin_amdgcn_permlane16_swap(__float_as_uint(x), __float_as_uint(x), false, false);
  x = fmaxf(__uint_as_float(s[0]), __uint_as_float(s[1]));
  auto t = __builtin_amdgcn_permlane32_swap(__float_as_uint(x), __float_as_uint(x), false, false);
  return fmaxf(__uint_as_float(t[0]), __uint_as_float(t[1]));
}
__device__ __forceinline__ float xrow_sum(float x) {
  auto s = __builtin_amdgcn_permlane16_swap(__float_as_uint(x), __float_as_uint(x), false, false);
  x = __uint_as_float(s[0]) + __uint_as_float(s[1]);
  auto t = __builtin_amdgcn_permlane32_swap(__float_as_uint(x), __float_as_uint(x), false, false);
  return __uint_as_float(t[0]) + __uint_as_float(t[1]);
}
__device__ __forceinline__ float row_sum(float x) { x += dpp<XOR1>(x); x += dpp<XOR2>(x); x += dpp<XOR7>(x); x += dpp<XOR8>(x); return x; }
__device__ __forceinline__ float row_max(float x) {
  x = fmaxf(x, dpp<XOR1>(x)); x = fmaxf(x, dpp<XOR2>(x)); x = fmaxf(x, dpp<XOR7>(x)); x = fmaxf(x, dpp<XOR8>(x)); return x;
}
__device__ __forceinline__ float wave_sum(float x) { return xrow_sum(row_sum(x)); }
__device__ __forceinline__ u32x4 ldnt(const void* p) { return __builtin_nontemporal_load(reinterpret_cast<const u32x4*>(p)); }
__device__ __forceinline__ float silu_f(float x) { return x / (1.f + __expf(-x)); }

struct Tag { unsigned lo, hi; };
__device__ __forceinline__ unsigned g_enc(float v, const Tag& t) { return ((__float_as_uint(v) + 8u) >> 4) | t.lo; }
__device__ __forceinline__ float g_dec(unsigned lo) { return __uint_as_float(lo << 4); }
__device__ __forceinline__ float g_round(float v) { return __uint_as_float(((__float_as_uint(v) + 8u) >> 4) << 4); }
__device__ __forceinline__ unsigned g_bad(unsigned lo, unsigned hi, const Tag& t) { return (hi ^ t.hi) | ((lo ^ t.lo) & 0xF0000000u); }
constexpr int AUX_SC1 = 16, AUX_NT = 2;
__device__ __forceinline__ void st1(__amdgpu_buffer_rsrc_t rs, unsigned off, float v, const Tag& t) {
  u32x2 x = {g_enc(v, t), t.hi};
  __builtin_amdgcn_raw_buffer_store_b64(x, rs, off, 0, AUX_SC1);
}
__device__ __forceinline__ void st2(__amdgpu_buffer_rsrc_t rs, unsigned off, float v0, float v1, const Tag& t) {
  u32x4 x = {g_enc(v0, t), t.hi, g_enc(v1, t), t.hi};
  __builtin_amdgcn_raw_buffer_store_b128(x, rs, off, 0, AUX_SC1);
}
__device__ __forceinline__ u32x4 ld2(__amdgpu_buffer_rsrc_t rs, unsigned off) { return __builtin_amdgcn_raw_buffer_load_b128(rs, off, 0, AUX_SC1); }
__device__ __forceinline__ unsigned g_bad2(const u32x4& x, const Tag& t) { return g_bad(x[0], x[1], t) | g_bad(x[2], x[3], t); }
#define POLL_FENCE() asm volatile("" ::: "memory")
template <int N, int STRIDE, int LAST, int SLEEP>
__device__ __forceinline__ void gather(__amdgpu_buffer_rsrc_t rs, unsigned off, const Tag& tag, u32x4 (&g)[N], unsigned cap) {
  unsigned bad = 1u;
  for (unsigned spins = 0;; ++spins) {
    POLL_FENCE();
    if (bad) {
#pragma unroll
      for (int k = 0; k < N; ++k) g[k] = ld2(rs, off + (unsigned)(k == N - 1 ? LAST : k * STRIDE));
      bad = 0u;
#pragma unroll
      for (int k = 0; k < N; ++k) bad |= g_bad2(g[k], tag);
    }
    if (__all(bad == 0u) || spins > cap) break;
    __builtin_amdgcn_s_sleep(SLEEP);
  }
}

struct Params {
  const unsigned short *x, *kc, *vc, *wq, *wo, *wg, *wu, *wd;
  unsigned short* out;
  unsigned char* ws;
};

constexpr int L_XN = 0;
constexpr int L_QP = L_XN + 576;
constexpr int L_Q = L_QP + 256;
constexpr int L_H = L_Q + 128;
constexpr int L_SYNC = L_H + 32;
constexpr int L_MRG = L_SYNC + 4;
constexpr int L_WO = L_MRG + 4 * 2 * 132;
constexpr int L_TOTAL = L_WO + 4608;

__device__ __forceinline__ unsigned claim_issue(const unsigned char* ctr) {
  unsigned v; u64 save;
  asm volatile("s_mov_b64 %1, exec\n\ts_mov_b64 exec, 1\n\tglobal_atomic_add %0, %2, %3, %4 sc0\n\ts_mov_b64 exec, %1"
               : "=&v"(v), "=&s"(save) : "v"(0u), "v"(1u), "s"(ctr) : "memory");
  return v;
}
__device__ __forceinline__ unsigned claim_get(unsigned v) {
  unsigned n;
  asm volatile("s_waitcnt vmcnt(8)\n\tv_readfirstlane_b32 %0, %1" : "=s"(n) : "v"(v) : "memory");
  return n;
}
__device__ __forceinline__ void lds_wait_ge(float* lds, int idx, unsigned want) {
  volatile LAS unsigned* w = (volatile LAS unsigned*)(lds + idx);
  for (unsigned spins = 0; *w < want && spins < (SPIN_CAP << 4); ++spins) __builtin_amdgcn_s_sleep(1);
  asm volatile("" ::: "memory");
}

struct StreamState {
  float m, l;
  f32x4 acc[8];
};

template <bool RELOAD>
__device__ __forceinline__ void tile_body(u32x4 (&K)[4], u32x4 (&V)[4], const u32x4& q0, const u32x4& q1, StreamState& st,
                                          __amdgpu_buffer_rsrc_t rk, __amdgpu_buffer_rsrc_t rv, unsigned vo, unsigned so, int c) {
  constexpr float SCALE = 0.088388347648318440f, LOG2E = 1.4426950408889634f;
  float x[8];
#pragma unroll
  for (int i = 0; i < 4; ++i) {
    float d0 = 0.f, d1 = 0.f;
#pragma unroll
    for (int k = 0; k < 4; ++k) { d0 = dot2(K[i][k], q0[k], d0); d1 = dot2(K[i][k], q1[k], d1); }
    x[2 * i] = d0; x[2 * i + 1] = d1;
    if (RELOAD) K[i] = __builtin_amdgcn_raw_buffer_load_b128(rk, vo + i * 1024, so, AUX_NT);
  }
#define TR_STEP(HALF, CTRL, BIT) _Pragma("unroll") for (int h = 0; h < (HALF); ++h) { \
    const float lo_ = x[h], hi_ = x[h + (HALF)]; const float t1_ = lo_ + dpp<CTRL>(lo_), t2_ = hi_ + dpp<CTRL>(hi_); \
    x[h] = (c & (BIT)) ? t2_ : t1_; }
  TR_STEP(4, XOR7, 4) TR_STEP(2, XOR2, 2) TR_STEP(1, XOR1, 1)
#undef TR_STEP
  x[0] += dpp<XOR8>(x[0]);
  const float s = rbf(rbf(x[0]) * SCALE);
  if (__any(s > st.m)) {
    float pm = s; pm = fmaxf(pm, dpp<ROR(2)>(pm)); pm = fmaxf(pm, dpp<ROR(4)>(pm)); pm = xrow_max(pm);
    const float mn = fmaxf(st.m, pm);
    const float alpha = __builtin_amdgcn_exp2f((st.m - mn) * LOG2E);
    st.m = mn; st.l *= alpha;
#pragma unroll
    for (int e = 0; e < 8; ++e) st.acc[e] *= alpha;
  }
  const float p = __builtin_amdgcn_exp2f((s - st.m) * LOG2E);
  st.l += p;
  const unsigned w1 = cvtpk(p, dpp<ROR(14)>(p));
  u32x2 pb;
  pb[0] = w1; pb[1] = dppu<ROR(12)>(w1);
#pragma unroll
  for (int k = 0; k < 4; ++k) {
    u32x2 alo, ahi;
#pragma unroll
    for (int j = 0; j < 2; ++j) {
      alo[j] = __builtin_amdgcn_perm(V[2 * j + 1][k], V[2 * j][k], 0x05040100u);
      ahi[j] = __builtin_amdgcn_perm(V[2 * j + 1][k], V[2 * j][k], 0x07060302u);
    }
    st.acc[2 * k] = __builtin_amdgcn_mfma_f32_16x16x16bf16_1k(__builtin_bit_cast(bf16x4, alo), __builtin_bit_cast(bf16x4, pb), st.acc[2 * k], 0, 0, 0);
    st.acc[2 * k + 1] = __builtin_amdgcn_mfma_f32_16x16x16bf16_1k(__builtin_bit_cast(bf16x4, ahi), __builtin_bit_cast(bf16x4, pb), st.acc[2 * k + 1], 0, 0, 0);
  }
  if (RELOAD) {
#pragma unroll
    for (int i = 0; i < 4; ++i) V[i] = __builtin_amdgcn_raw_buffer_load_b128(rv, vo + i * 1024, so, AUX_NT);
  }
}

__global__ void __launch_bounds__(512, 2) fused_kernel(Params p) {
  __shared__ __attribute__((aligned(16))) float lds[L_TOTAL];
  const int tid = threadIdx.x, lane = tid & 63, wave = __builtin_amdgcn_readfirstlane(tid >> 6);
  const int b = blockIdx.x, kvh = b >> 4, split = b & 15;
  unsigned char* ws = p.ws;
  const __amdgpu_buffer_rsrc_t rs = __builtin_amdgcn_make_buffer_rsrc(ws, 0, (int)WS_END, 0x00020000);
  STAMP(0);

  if (wave < 4) {
    const int r = lane >> 4, c = lane & 15;
    constexpr unsigned TB = TILE * DH * 2, STEP = 4 * TB;
    const __amdgpu_buffer_rsrc_t rk = __builtin_amdgcn_make_buffer_rsrc((void*)(p.kc + (size_t)kvh * SK * DH), 0, SK * DH * 2, 0x00020000);
    const __amdgpu_buffer_rsrc_t rv = __builtin_amdgcn_make_buffer_rsrc((void*)(p.vc + (size_t)kvh * SK * DH), 0, SK * DH * 2, 0x00020000);
    const unsigned vo = (unsigned)lane * 16u;
    unsigned so = (unsigned)(split * TPC + wave) * TB;
    u32x4 K[NPRE][4], V[NPRE][4];
    __syncthreads();
#pragma unroll
    for (int t = 0; t < NPRE; ++t) {
      if (t > 0) asm volatile("s_waitcnt vmcnt(4)" ::: "memory");
#pragma unroll
      for (int i = 0; i < 4; ++i) K[t][i] = __builtin_amdgcn_raw_buffer_load_b128(rk, vo + i * 1024, so + t * STEP, AUX_NT);
      if (t > 0) asm volatile("s_waitcnt vmcnt(4)" ::: "memory");
#pragma unroll
      for (int i = 0; i < 4; ++i) V[t][i] = __builtin_amdgcn_raw_buffer_load_b128(rv, vo + i * 1024, so + t * STEP, AUX_NT);
    }
    asm volatile("" ::: "memory");
    __builtin_amdgcn_sched_barrier(0);
    lds_wait_ge(lds, L_SYNC + 3, 2u);
    STAMP(1);
    const u32x4 q0 = *reinterpret_cast<const u32x4*>(reinterpret_cast<const unsigned short*>(lds + L_Q) + c * 8);
    const u32x4 q1 = *reinterpret_cast<const u32x4*>(reinterpret_cast<const unsigned short*>(lds + L_Q) + DH + c * 8);
    StreamState st;
    st.m = -INFINITY; st.l = 0.f;
#pragma unroll
    for (int e = 0; e < 8; ++e) st.acc[e] = f32x4{0.f, 0.f, 0.f, 0.f};
    const unsigned char* ctr = ws + WS_CTR + kvh * 256;

    so += NPRE * STEP;
    tile_body<true>(K[0], V[0], q0, q1, st, rk, rv, vo, so, c);
#pragma unroll
    for (int t = 1; t < NPRE; ++t) tile_body<false>(K[t], V[t], q0, q1, st, rk, rv, vo, so, c);
    for (int it = NPRE; it < NSTATIC - 2; ++it) {
      so += STEP;
      tile_body<true>(K[0], V[0], q0, q1, st, rk, rv, vo, so, c);
    }
    unsigned cv = claim_issue(ctr);
    so += STEP;
    tile_body<true>(K[0], V[0], q0, q1, st, rk, rv, vo, so, c);
    unsigned n = claim_get(cv);
    STAMP(2);
    while (n < (unsigned)POOL_N) {
      cv = claim_issue(ctr);
      so = ((n & 15u) * TPC + 4 * NSTATIC + (n >> 4)) * TB;
      tile_body<true>(K[0], V[0], q0, q1, st, rk, rv, vo, so, c);
      n = claim_get(cv);
    }
    tile_body<false>(K[0], V[0], q0, q1, st, rk, rv, vo, so, c);
    STAMP(3);
    float l = st.l;
    l += dpp<XOR2>(l); l += dpp<ROR(4)>(l); l = xrow_sum(l);
    float* mrg = lds + L_MRG;
    if (c < 2) {
      float* rec = mrg + (wave * 2 + c) * 132;
#pragma unroll
      for (int i = 0; i < 4; ++i) {
        *reinterpret_cast<f32x4*>(rec + 4 + 8 * (4 * r + i)) = f32x4{st.acc[0][i], st.acc[1][i], st.acc[2][i], st.acc[3][i]};
        *reinterpret_cast<f32x4*>(rec + 4 + 8 * (4 * r + i) + 4) = f32x4{st.acc[4][i], st.acc[5][i], st.acc[6][i], st.acc[7][i]};
      }
      if (r == 0) { rec[0] = st.m; rec[1] = l; }
    }
    __syncthreads();
    STAMP(4);
    if (wave < 2) {
      const int g = wave;
      Tag tag = {__float_as_uint(lds[L_SYNC]), __float_as_uint(lds[L_SYNC + 1])};
      float M = -INFINITY;
#pragma unroll
      for (int w = 0; w < 4; ++w) M = fmaxf(M, mrg[(w * 2 + g) * 132]);
      M = g_round(M);
      float L = 0.f, o0 = 0.f, o1 = 0.f;
#pragma unroll
      for (int w = 0; w < 4; ++w) {
        const float* rec = mrg + (w * 2 + g) * 132;
        const float wt = __builtin_amdgcn_exp2f((rec[0] - M) * 1.4426950408889634f);
        L = fmaf(wt, rec[1], L); o0 = fmaf(wt, rec[4 + 2 * lane], o0); o1 = fmaf(wt, rec[5 + 2 * lane], o1);
      }
      const unsigned slot = (unsigned)WS_PG + (unsigned)(((kvh * 8 + g * 4 + (lane >> 4)) * 16 + split) * 34) * 8u;
      st2(rs, slot + (unsigned)(lane & 15) * 16u, o0, o1, tag);
      if ((lane & 15) == 0) st2(rs, slot + 32u * 8u, M, L, tag);
      STAMP(5);
    }
    return;
  }

  const int sv = wave - 4;
  __builtin_amdgcn_s_setprio(3);
  const int cb = b >> 2, rq = b & 3;
  const u64 epoch = __hip_atomic_load((GAS u64*)(ws + WS_EPOCH), __ATOMIC_RELAXED, __HIP_MEMORY_SCOPE_AGENT);
  unsigned xw[5];
#pragma unroll
  for (int i = 0; i < 5; ++i) xw[i] = reinterpret_cast<const unsigned*>(p.x)[(i < 4 ? lane : (lane & 31)) + 64 * i];
  u32x4 wqv[5];
  unsigned short xr[5];
  const int row0 = rq * 144 + sv * 36 + (lane >> 3);
#pragma unroll
  for (int i = 0; i < 5; ++i) {
    const int row = row0 + 8 * i - ((i == 4 && lane >= 32) ? 4 : 0);
    wqv[i] = ldnt(p.wq + (size_t)row * 4096 + cb * 64 + (lane & 7) * 8);
    xr[i] = p.x[row];
  }
  if (split == 0 && tid == 256) __hip_atomic_store((GAS unsigned*)(ws + WS_CTR + kvh * 256), 0u, __ATOMIC_RELAXED, __HIP_MEMORY_SCOPE_AGENT);
  if (tid == 256) { lds[L_SYNC + 2] = 0.f; lds[L_SYNC + 3] = 0.f; }
  __builtin_amdgcn_sched_barrier(0);
  __syncthreads();
  __builtin_amdgcn_sched_barrier(0);
  if (lane >= 32) xw[4] = 0u;
  Tag tag;
  { const u64 t = (epoch + 1ull) & ((1ull << 36) - 1ull); tag.lo = (unsigned)(t & 15ull) << 28; tag.hi = (unsigned)(t >> 4); }
  float m_b, den, rden;
  {
    float s = 0.f;
#pragma unroll
    for (int i = 0; i < 5; ++i) s += bflo(xw[i]) + bfhi(xw[i]);
    s = wave_sum(s);
    const float mean = s * (1.f / D);
    float ss = 0.f;
#pragma unroll
    for (int i = 0; i < 5; ++i) {
      const float d0 = bflo(xw[i]) - mean, d1 = bfhi(xw[i]) - mean;
      ss += (i < 4 || lane < 32) ? d0 * d0 + d1 * d1 : 0.f;
    }
    ss = wave_sum(ss);
    m_b = rbf(mean);
    den = rbf(sqrtf(rbf(rbf(ss * (1.f / D)) + 1e-5f)));
    rden = __builtin_amdgcn_rcpf(den);
  }
  auto norm1 = [&](float xv) {
    const float a = rbf(xv - m_b), q = a * rden;
    return rbf(fmaf(fmaf(-q, den, a), rden, q));
  };
  {
    float a[8] = {0, 0, 0, 0, 0, 0, 0, 0};
#pragma unroll
    for (int i = 0; i < 5; ++i) {
      const float xv = (i < 4 || lane < 32) ? norm1(bf2f(xr[i])) : 0.f;
#pragma unroll
      for (int k = 0; k < 4; ++k) { a[2 * k] = fmaf(xv, bflo(wqv[i][k]), a[2 * k]); a[2 * k + 1] = fmaf(xv, bfhi(wqv[i][k]), a[2 * k + 1]); }
    }
#pragma unroll
    for (int h = 0; h < 4; ++h) {
      auto s = __builtin_amdgcn_permlane32_swap(__float_as_uint(a[h]), __float_as_uint(a[h + 4]), false, false);
      a[h] = __uint_as_float(s[0]) + __uint_as_float(s[1]);
    }
#pragma unroll
    for (int h = 0; h < 2; ++h) {
      auto s = __builtin_amdgcn_permlane16_swap(__float_as_uint(a[h]), __float_as_uint(a[h + 2]), false, false);
      a[h] = __uint_as_float(s[0]) + __uint_as_float(s[1]);
    }
    const float t1 = a[0] + dpp<XOR8>(a[0]), t2 = a[1] + dpp<XOR8>(a[1]);
    lds[L_QP + sv * 64 + (lane & 7) * 8 + (lane >> 3)] = (lane & 8) ? t2 : t1;
  }
  if (tid == 256) { lds[L_SYNC] = __uint_as_float(tag.lo); lds[L_SYNC + 1] = __uint_as_float(tag.hi); }
  asm volatile("s_waitcnt vmcnt(0) lgkmcnt(0)" ::: "memory");
  unsigned arr = 0;
  if (lane == 0) arr = __hip_atomic_fetch_add((LAS unsigned*)(lds + L_SYNC + 2), 1u, __ATOMIC_RELAXED, __HIP_MEMORY_SCOPE_WORKGROUP);
  arr = __builtin_amdgcn_readfirstlane(arr);
  asm volatile("" ::: "memory");
  if (arr == 3u) {
    const float s = (lds[L_QP + lane] + lds[L_QP + 64 + lane]) + (lds[L_QP + 128 + lane] + lds[L_QP + 192 + lane]);
    st1(rs, (unsigned)WS_QG + (unsigned)(b * 64 + lane) * 8u, s, tag);
  }
  STAMP(1);

  const int rg = split >> 1, ch = split & 1;
  const int f7 = kvh * 7 + split - 9;
  const bool is_final = split >= 9 && f7 < 72;
  float gate0 = 0.f, gate1 = 0.f;

  if (sv == 0) {
    lds_wait_ge(lds, L_SYNC + 3, 2u);
    const unsigned short* wsrc = p.wo + (size_t)(kvh * 256 + rg * 32) * D + ch * 288;
#pragma unroll
    for (int rd = 0; rd < 3; ++rd) {
      const int blk = min(rd * 64 + lane, 143), h = blk / 36, cg = blk % 36;
      u32x4 w[8];
#pragma unroll
      for (int i = 0; i < 8; ++i) w[i] = ldnt(wsrc + (size_t)(8 * h + i) * D + cg * 8);
      u32x4* dst = reinterpret_cast<u32x4*>(lds + L_WO) + (cg >> 1) * 64 + h * 16 + (cg & 1) * 8;
#pragma unroll
      for (int k = 0; k < 4; ++k) {
        u32x4 lo, hi;
#pragma unroll
        for (int j = 0; j < 4; ++j) {
          lo[j] = __builtin_amdgcn_perm(w[2 * j + 1][k], w[2 * j][k], 0x05040100u);
          hi[j] = __builtin_amdgcn_perm(w[2 * j + 1][k], w[2 * j][k], 0x07060302u);
        }
        dst[2 * k] = lo; dst[2 * k + 1] = hi;
      }
      asm volatile("" ::: "memory");
    }
  } else if (sv == 1) {
    if (split < 9) {
      const int u = kvh * 9 + split;
      const __amdgpu_buffer_rsrc_t rg_ = __builtin_amdgcn_make_buffer_rsrc((void*)p.wg, 0, D * F * 2, 0x00020000);
      const __amdgpu_buffer_rsrc_t ru_ = __builtin_amdgcn_make_buffer_rsrc((void*)p.wu, 0, D * F * 2, 0x00020000);
      const __amdgpu_buffer_rsrc_t rd_ = __builtin_amdgcn_make_buffer_rsrc((void*)p.wd, 0, D * F * 2, 0x00020000);
      const unsigned gofs = (unsigned)((lane >> 2) * F + u * 32 + (lane & 3) * 8) * 2u;
      const unsigned dofs = (unsigned)(u * 32 * D + lane * 8) * 2u, dofs2 = (unsigned)((u * 32 + (lane >> 3)) * D + 512 + (lane & 7) * 8) * 2u;
#pragma unroll
      for (int i = 0; i < 5; ++i)
        if (i < 4 || lane < 32) *reinterpret_cast<float2*>(lds + L_XN + 2 * (lane + 64 * i)) = make_float2(norm1(bflo(xw[i])), norm1(bfhi(xw[i])));
      float xv[36];
#pragma unroll
      for (int i = 0; i < 36; ++i) xv[i] = lds[L_XN + i * 16 + (lane >> 2)];
      lds_wait_ge(lds, L_SYNC + 3, 2u);
      auto fload = [&](int s) -> u32x4 {
        return s < 36 ? __builtin_amdgcn_raw_buffer_load_b128(rg_, gofs, s * 16 * F * 2, AUX_NT)
             : s < 72 ? __builtin_amdgcn_raw_buffer_load_b128(ru_, gofs, (s - 36) * 16 * F * 2, AUX_NT)
             : s < 104 ? __builtin_amdgcn_raw_buffer_load_b128(rd_, dofs, (s - 72) * D * 2, AUX_NT)
                       : __builtin_amdgcn_raw_buffer_load_b128(rd_, dofs2, (s - 104) * 8 * D * 2, AUX_NT);
      };
      u32x4 g[FRING];
#pragma unroll
      for (int i = 0; i < FRING; ++i) g[i] = fload(i);
      float a[8] = {0, 0, 0, 0, 0, 0, 0, 0}, bb[8] = {0, 0, 0, 0, 0, 0, 0, 0};
#define PIN8(v) asm volatile("" : "+v"(v[0]), "+v"(v[1]), "+v"(v[2]), "+v"(v[3]), "+v"(v[4]), "+v"(v[5]), "+v"(v[6]), "+v"(v[7]) :: "memory")
#define FMA8(acc, sc, w) _Pragma("unroll") for (int k = 0; k < 4; ++k) { acc[2 * k] = fmaf(sc, bflo(w[k]), acc[2 * k]); acc[2 * k + 1] = fmaf(sc, bfhi(w[k]), acc[2 * k + 1]); }
#pragma unroll
      for (int s = 0; s < 36; ++s) { FMA8(a, xv[s], g[s % FRING]); PIN8(a); g[s % FRING] = fload(s + FRING); }
#pragma unroll
      for (int s = 36; s < 72; ++s) { FMA8(bb, xv[s - 36], g[s % FRING]); PIN8(bb); g[s % FRING] = fload(s + FRING); }
      float hreg[8];
#pragma unroll
      for (int k = 0; k < 8; ++k) {
        a[k] += dpp<ROR(4)>(a[k]); a[k] += dpp<ROR(8)>(a[k]); a[k] = xrow_sum(a[k]);
        bb[k] += dpp<ROR(4)>(bb[k]); bb[k] += dpp<ROR(8)>(bb[k]); bb[k] = xrow_sum(bb[k]);
        hreg[k] = rbf(rbf(silu_f(rbf(a[k]))) * rbf(bb[k]));
        if (lane < 4) lds[L_H + lane * 8 + k] = hreg[k];
      }
      float hx[4];
#pragma unroll
      for (int o = 0; o < 4; ++o) hx[o] = lds[L_H + o * 8 + (lane >> 3)];
      float y[8] = {0, 0, 0, 0, 0, 0, 0, 0}, z[8] = {0, 0, 0, 0, 0, 0, 0, 0};
#pragma unroll
      for (int s = 72; s < 104; ++s) {
        const float hv = __uint_as_float(__builtin_amdgcn_readlane(__float_as_uint(hreg[(s - 72) & 7]), (s - 72) >> 3));
        FMA8(y, hv, g[s % FRING]); PIN8(y);
        if (s + FRING < 108) g[s % FRING] = fload(s + FRING);
      }
#pragma unroll
      for (int s = 104; s < 108; ++s) { FMA8(z, hx[s - 104], g[s % FRING]); }
#pragma unroll
      for (int k = 0; k < 8; ++k) { z[k] += dpp<XOR8>(z[k]); z[k] = xrow_sum(z[k]); }
      const unsigned yo = (unsigned)WS_YG + (unsigned)(lane * 144 + u) * 64u;
#pragma unroll
      for (int k = 0; k < 4; ++k) st2(rs, yo + (unsigned)k * 16u, y[2 * k], y[2 * k + 1], tag);
      if (lane < 8) {
        const unsigned zo = (unsigned)WS_YG + (unsigned)((64 + lane) * 144 + u) * 64u;
#pragma unroll
        for (int k = 0; k < 4; ++k) st2(rs, zo + (unsigned)k * 16u, z[2 * k], z[2 * k + 1], tag);
      }
    }
  } else {
    {
      const unsigned off = (unsigned)WS_QG + (unsigned)((kvh * 16 + (sv - 2) * 8 + (lane >> 5) * 4) * 64 + (lane & 31) * 2) * 8u;
      u32x4 g4[4];
      gather<4, 512, 1536, 1>(rs, off, tag, g4, SPIN_CAP);
      const float qa = (g_dec(g4[0][0]) + g_dec(g4[1][0])) + (g_dec(g4[2][0]) + g_dec(g4[3][0]));
      const float qb = (g_dec(g4[0][2]) + g_dec(g4[1][2])) + (g_dec(g4[2][2]) + g_dec(g4[3][2]));
      reinterpret_cast<unsigned*>(lds + L_Q)[(sv - 2) * 64 + lane] = cvtpk(qa, qb);
      asm volatile("s_waitcnt lgkmcnt(0)" ::: "memory");
      if (lane == 0) __hip_atomic_fetch_add((LAS unsigned*)(lds + L_SYNC + 3), 1u, __ATOMIC_RELAXED, __HIP_MEMORY_SCOPE_WORKGROUP);
    }
    STAMP(2);
    if (sv == 2 && is_final) {
      const unsigned off = (unsigned)WS_YG + (unsigned)f7 * (144u * 64u) + (unsigned)lane * 16u;
      u32x4 g9[9];
      for (int i = 0; i < 8; ++i) __builtin_amdgcn_s_sleep(127);
      gather<9, 1024, 8192, 127>(rs, off, tag, g9, SPIN_CAP >> 6);
      float y0 = 0.f, y1 = 0.f;
#pragma unroll
      for (int k = 0; k < 9; ++k) { y0 += g_dec(g9[k][0]); y1 += g_dec(g9[k][2]); }
      y0 += dpp<ROR(4)>(y0); y0 += dpp<ROR(8)>(y0); y0 = xrow_sum(y0);
      y1 += dpp<ROR(4)>(y1); y1 += dpp<ROR(8)>(y1); y1 = xrow_sum(y1);
      gate0 = rbf(silu_f(rbf(y0))); gate1 = rbf(silu_f(rbf(y1)));
    }
  }
  STAMP(3);
  __syncthreads();
  STAMP(4);
  if (sv < 2) {
    const int R = lane >> 4, c = lane & 15;
    u32x4 bfr[9];
#pragma unroll
    for (int j = 0; j < 9; ++j) bfr[j] = *reinterpret_cast<const u32x4*>(lds + L_WO + ((sv * 9 + j) * 64 + lane) * 4);
    const unsigned off = (unsigned)WS_PG + (unsigned)(((kvh * 8 + rg) * 16 + c) * 34) * 8u;
    const int col0 = ch * 288 + (sv * 9 + R) * 16 + c;
    const unsigned so0 = (unsigned)WS_SG + (unsigned)(((col0 >> 3) * 128 + kvh * 8 + rg) * 8 + (col0 & 7)) * 8u;
    u32x4 g5[5];
    {
      unsigned bad = 1u;
      for (unsigned spins = 0;; ++spins) {
        POLL_FENCE();
        if (bad) {
#pragma unroll
          for (int k = 0; k < 4; ++k) g5[k] = ld2(rs, off + (unsigned)(R * 64 + k * 16));
          g5[4] = ld2(rs, off + 256u);
          bad = 0u;
#pragma unroll
          for (int k = 0; k < 5; ++k) bad |= g_bad2(g5[k], tag);
        }
        if (__all(bad == 0u) || spins > SPIN_CAP) break;
        __builtin_amdgcn_s_sleep(1);
      }
    }
    STAMP(5);
    const float ms = g_dec(g5[4][0]), ls = g_dec(g5[4][2]);
    const float M = row_max(ms);
    const float w = __builtin_amdgcn_exp2f((ms - M) * 1.4426950408889634f);
    const float L = row_sum(w * ls);
    float inv = __builtin_amdgcn_rcpf(L);
    inv = fmaf(fmaf(-L, inv, 1.f), inv, inv);
    u32x4 a;
#pragma unroll
    for (int k = 0; k < 4; ++k) {
      const float o0 = row_sum(w * g_dec(g5[k][0])) * inv, o1 = row_sum(w * g_dec(g5[k][2])) * inv;
      a[k] = cvtpk(o0, o1);
    }
    auto mm = [&](const u32x4& bf) -> float {
      const f32x4 d = __builtin_amdgcn_mfma_f32_16x16x32_bf16(__builtin_bit_cast(bf16x8, a), __builtin_bit_cast(bf16x8, bf), f32x4{0.f, 0.f, 0.f, 0.f}, 0, 0, 0);
      return d[0];
    };
    const bool r1 = (R & 1) != 0, r2 = (R & 2) != 0;
    const float e0 = mm(bfr[0]), e1 = mm(bfr[1]), e2 = mm(bfr[2]), e3 = mm(bfr[3]);
    const float e4 = mm(bfr[4]), e5 = mm(bfr[5]), e6 = mm(bfr[6]), e7 = mm(bfr[7]), e8 = mm(bfr[8]);
    st1(rs, so0, r2 ? (r1 ? e3 : e2) : (r1 ? e1 : e0), tag);
    st1(rs, so0 + 8u * 128u * 64u, r2 ? (r1 ? e7 : e6) : (r1 ? e5 : e4), tag);
    if (R == 0) st1(rs, so0 + 2u * (8u * 128u * 64u), e8, tag);
    STAMP(6);
  } else if (sv == 2 && is_final) {
    const unsigned xab = reinterpret_cast<const unsigned*>(p.x)[f7 * 4 + (lane & 3)];
    const unsigned off = (unsigned)WS_SG + (unsigned)f7 * (128u * 64u) + (unsigned)lane * 16u;
    u32x4 g8[8];
    gather<8, 1024, 7168, 1>(rs, off, tag, g8, SPIN_CAP);
    STAMP(5);
    float s0 = 0.f, s1 = 0.f;
#pragma unroll
    for (int k = 0; k < 8; ++k) { s0 += g_dec(g8[k][0]); s1 += g_dec(g8[k][2]); }
    s0 += dpp<ROR(4)>(s0); s0 += dpp<ROR(8)>(s0); s0 = xrow_sum(s0);
    s1 += dpp<ROR(4)>(s1); s1 += dpp<ROR(8)>(s1); s1 = xrow_sum(s1);
    const float o0 = bflo(xab) + rbf(rbf(s0) * gate0), o1 = bfhi(xab) + rbf(rbf(s1) * gate1);
    if (lane < 4) reinterpret_cast<unsigned*>(p.out)[f7 * 4 + lane] = cvtpk(o0, o1);
    if (f7 == 0 && lane == 0) __hip_atomic_store((GAS u64*)(ws + WS_EPOCH), epoch + 1ull, __ATOMIC_RELAXED, __HIP_MEMORY_SCOPE_AGENT);
    STAMP(6);
  }
}

extern "C" void kernel_launch(void* const* d_in, const int* in_sizes, int n_in, void* d_out, int out_size, void* d_ws, size_t ws_size,
                              hipStream_t stream) {
  Params p;
  p.x = (const unsigned short*)d_in[0];
  p.kc = (const unsigned short*)d_in[1];
  p.vc = (const unsigned short*)d_in[2];
  p.wq = (const unsigned short*)d_in[3];
  p.wo = (const unsigned short*)d_in[4];
  p.wg = (const unsigned short*)d_in[6];
  p.wu = (const unsigned short*)d_in[7];
  p.wd = (const unsigned short*)d_in[8];
  p.out = (unsigned short*)d_out;
  p.ws = (unsigned char*)d_ws;
  fused_kernel<<<256, 512, 0, stream>>>(p);
}
```

```cpp
#include <hip/hip_runtime.h>
#include <hip/hip_bf16.h>
#include <cstdint>
#include <type_traits>

typedef unsigned u32x4 __attribute__((ext_vector_type(4)));
typedef unsigned u32x2 __attribute__((ext_vector_type(2)));
typedef float f32x4 __attribute__((ext_vector_type(4)));
typedef float f32x2 __attribute__((ext_vector_type(2)));
typedef short bf16x8 __attribute__((ext_vector_type(8)));
typedef short bf16x4 __attribute__((ext_vector_type(4)));
typedef __bf16 bf16x2 __attribute__((ext_vector_type(2)));
typedef unsigned long long u64;
#define GAS __attribute__((address_space(1)))
#define LAS __attribute__((address_space(3)))

constexpr int D = 576, SK = 65536, DH = 128, F = 4608;
constexpr int TILE = 16;
constexpr unsigned TAU0 = 938, TAU1 = 855, WSL = 6;
constexpr int NPK = 8;
constexpr int PWIN = 8;
constexpr int RPI = 4, NRG = 2;
constexpr int LPS = 64 / RPI, NCB = 9 * RPI, GCOLS = 8 * LPS;
constexpr int NL1 = 144 / (RPI * NRG);
constexpr int T1 = 2 * NCB * NRG / 8;
static_assert(GCOLS == 128 && T1 <= 23, "");
constexpr unsigned SPIN_CAP = 1u << 20;

constexpr size_t WS_EPOCH = 0;
constexpr size_t WS_T0 = 4096;
constexpr size_t WS_QG = 16384;
constexpr size_t WS_AG = WS_QG + 64 * 4 * 64 * 8;
constexpr size_t WS_YG = WS_AG + 2 * NRG * F * 8;
constexpr size_t WS_PG = WS_YG + 72 * 576 * 8;
constexpr size_t WS_SG = WS_PG + 256 * 2 * 144 * 8;
constexpr size_t WS_END = WS_SG + 128 * 576 * 8;

__device__ __forceinline__ float bf2f(unsigned short b) { return __builtin_bit_cast(float, (unsigned)b << 16); }
__device__ __forceinline__ float bflo(unsigned w) { return __builtin_bit_cast(float, w << 16); }
__device__ __forceinline__ float bfhi(unsigned w) { return __builtin_bit_cast(float, w & 0xffff0000u); }
__device__ __forceinline__ unsigned cvtpk(float lo, float hi) {
  return __builtin_bit_cast(unsigned, __builtin_convertvector(f32x2{lo, hi}, bf16x2));
}
__device__ __forceinline__ float rbf(float x) { return bflo(cvtpk(x, x)); }
__device__ __forceinline__ float dot2(unsigned k, unsigned q, float acc) {
  return __builtin_amdgcn_fdot2_f32_bf16(__builtin_bit_cast(bf16x2, k), __builtin_bit_cast(bf16x2, q), acc, false);
}
template <int CTRL> __device__ __forceinline__ float dpp(float x) {
  return __builtin_bit_cast(float, __builtin_amdgcn_mov_dpp(__builtin_bit_cast(int, x), CTRL, 0xf, 0xf, true));
}
template <int CTRL> __device__ __forceinline__ unsigned dppu(unsigned x) {
  return (unsigned)__builtin_amdgcn_mov_dpp((int)x, CTRL, 0xf, 0xf, true);
}
constexpr int XOR1 = 0xB1, XOR2 = 0x4E, XOR7 = 0x141, XOR8 = 0x128;
constexpr int ROR(int n) { return 0x120 + n; }
__device__ __forceinline__ float xrow_max(float x) {
  auto s = __builtin_amdgcn_permlane16_swap(__float_as_uint(x), __float_as_uint(x), false, false);
  x = fmaxf(__uint_as_float(s[0]), __uint_as_float(s[1]));
  auto t = __builtin_amdgcn_permlane32_swap(__float_as_uint(x), __float_as_uint(x), false, false);
  return fmaxf(__uint_as_float(t[0]), __uint_as_float(t[1]));
}
__device__ __forceinline__ float xrow_sum(float x) {
  auto s = __builtin_amdgcn_permlane16_swap(__float_as_uint(x), __float_as_uint(x), false, false);
  x = __uint_as_float(s[0]) + __uint_as_float(s[1]);
  auto t = __builtin_amdgcn_permlane32_swap(__float_as_uint(x), __float_as_uint(x), false, false);
  return __uint_as_float(t[0]) + __uint_as_float(t[1]);
}
__device__ __forceinline__ float row_sum(float x) { x += dpp<XOR1>(x); x += dpp<XOR2>(x); x += dpp<XOR7>(x); x += dpp<XOR8>(x); return x; }
__device__ __forceinline__ float row_max(float x) {
  x = fmaxf(x, dpp<XOR1>(x)); x = fmaxf(x, dpp<XOR2>(x)); x = fmaxf(x, dpp<XOR7>(x)); x = fmaxf(x, dpp<XOR8>(x)); return x;
}
__device__ __forceinline__ float wave_sum(float x) { return xrow_sum(row_sum(x)); }
__device__ __forceinline__ u32x4 ldnt(const void* p) { return __builtin_nontemporal_load(reinterpret_cast<const u32x4*>(p)); }
__device__ __forceinline__ float silu_f(float x) { return x / (1.f + __expf(-x)); }

struct Tag { unsigned lo, hi; };
__device__ __forceinline__ unsigned g_enc(float v, const Tag& t) { return ((__float_as_uint(v) + 8u) >> 4) | t.lo; }
__device__ __forceinline__ float g_dec(unsigned lo) { return __uint_as_float(lo << 4); }
__device__ __forceinline__ unsigned g_bad(unsigned lo, unsigned hi, const Tag& t) { return (hi ^ t.hi) | ((lo ^ t.lo) & 0xF0000000u); }
constexpr int AUX_SC1 = 16, AUX_NT = 2;
__device__ __forceinline__ void st1(__amdgpu_buffer_rsrc_t rs, unsigned off, float v, const Tag& t) {
  u32x2 x = {g_enc(v, t), t.hi};
  __builtin_amdgcn_raw_buffer_store_b64(x, rs, off, 0, AUX_SC1);
}
__device__ __forceinline__ void st2(__amdgpu_buffer_rsrc_t rs, unsigned off, float v0, float v1, const Tag& t) {
  u32x4 x = {g_enc(v0, t), t.hi, g_enc(v1, t), t.hi};
  __builtin_amdgcn_raw_buffer_store_b128(x, rs, off, 0, AUX_SC1);
}
__device__ __forceinline__ u32x4 ld2(__amdgpu_buffer_rsrc_t rs, unsigned off) { return __builtin_amdgcn_raw_buffer_load_b128(rs, off, 0, AUX_SC1); }
__device__ __forceinline__ unsigned g_bad2(const u32x4& x, const Tag& t) { return g_bad(x[0], x[1], t) | g_bad(x[2], x[3], t); }
#define MEM_FENCE() asm volatile("" ::: "memory")
template <int N, int STRIDE, int SLEEP>
__device__ __forceinline__ unsigned gather(__amdgpu_buffer_rsrc_t rs, unsigned off, unsigned offl, const Tag& tag, u32x4 (&g)[N], unsigned cap) {
  unsigned bad = 1u;
  for (unsigned spins = 0;; ++spins) {
    MEM_FENCE();
    if (bad) {
#pragma unroll
      for (int k = 0; k < N; ++k) g[k] = ld2(rs, k == N - 1 ? offl : off + (unsigned)(k * STRIDE));
      bad = 0u;
#pragma unroll
      for (int k = 0; k < N; ++k) bad |= g_bad2(g[k], tag);
    }
    if (__all(bad == 0u) || spins > cap) return spins;
    __builtin_amdgcn_s_sleep(SLEEP);
  }
}

struct Params {
  const unsigned short *x, *kc, *vc, *wq, *wo, *wg, *wu, *wd;
  unsigned short* out;
  unsigned char* ws;
};

constexpr int L_XN = 0;
constexpr int L_QP = L_XN + 576;
constexpr int L_Q = L_QP + 256;
constexpr int L_Y = L_Q + 128;
constexpr int L_SYNC = L_Y + 576 + 64;
constexpr int L_MRG = L_SYNC + 12;
constexpr int L_GU = L_MRG + 4 * 2 * 132;
constexpr int L_WO = L_GU + 4 * 512;
constexpr int L_WOR = L_WO + 4608;
constexpr int L_WD = L_WOR + 4608;
constexpr int L_SC = L_WD + 18432;
constexpr int L_TOTAL = L_SC + 4 * 64 * 16;

__device__ __forceinline__ void lds_wait_ge(float* lds, int idx, unsigned want) {
  volatile LAS unsigned* w = (volatile LAS unsigned*)(lds + idx);
  for (unsigned spins = 0; *w < want && spins < (SPIN_CAP << 4); ++spins) __builtin_amdgcn_s_sleep(1);
  MEM_FENCE();
}
__device__ __forceinline__ void lds_signal(float* lds, int idx, int lane) {
  asm volatile("s_waitcnt lgkmcnt(0)" ::: "memory");
  if (lane == 0) __hip_atomic_fetch_add((LAS unsigned*)(lds + idx), 1u, __ATOMIC_RELAXED, __HIP_MEMORY_SCOPE_WORKGROUP);
}
__device__ __forceinline__ float treduce8(float (&a)[8], int lane) {
#pragma unroll
  for (int h = 0; h < 4; ++h) {
    auto s = __builtin_amdgcn_permlane32_swap(__float_as_uint(a[h]), __float_as_uint(a[h + 4]), false, false);
    a[h] = __uint_as_float(s[0]) + __uint_as_float(s[1]);
  }
#pragma unroll
  for (int h = 0; h < 2; ++h) {
    auto s = __builtin_amdgcn_permlane16_swap(__float_as_uint(a[h]), __float_as_uint(a[h + 2]), false, false);
    a[h] = __uint_as_float(s[0]) + __uint_as_float(s[1]);
  }
  const float t1 = a[0] + dpp<XOR8>(a[0]), t2 = a[1] + dpp<XOR8>(a[1]);
  return (lane & 8) ? t2 : t1;
}
#define PIN8(v) asm volatile("" : "+v"(v[0]), "+v"(v[1]), "+v"(v[2]), "+v"(v[3]), "+v"(v[4]), "+v"(v[5]), "+v"(v[6]), "+v"(v[7]) :: "memory")
#define FMA8(acc, sc, w) _Pragma("unroll") for (int k_ = 0; k_ < 4; ++k_) { acc[2 * k_] = fmaf(sc, bflo(w[k_]), acc[2 * k_]); acc[2 * k_ + 1] = fmaf(sc, bfhi(w[k_]), acc[2 * k_ + 1]); }

template <int I, int E, typename Fn> __device__ __forceinline__ void static_for(Fn&& f) {
  if constexpr (I < E) { f(std::integral_constant<int, I>{}); static_for<I + 1, E>(f); }
}

__device__ __forceinline__ void pace(unsigned t0, unsigned slot) {
  const unsigned due = (slot * TAU0 - (slot * slot * (TAU0 - TAU1)) / 262u) >> 4;
  const unsigned stag = (((blockIdx.x >> 3) & 31u) * min(132u - slot, 64u) * 112u) >> 11;
  while ((unsigned)__builtin_amdgcn_s_memrealtime() - t0 < due + stag) __builtin_amdgcn_s_sleep(1);
}

struct StreamState {
  float m, l;
  f32x4 acc[8];
};

template <bool RELOAD>
__device__ __forceinline__ void step_k(u32x4 (&X)[4], const u32x4& q0, const u32x4& q1, StreamState& st, unsigned short* sc_slot,
                                       __amdgpu_buffer_rsrc_t rx, unsigned vo, unsigned off, int c, unsigned t0, unsigned slot) {
  constexpr float SCALE = 0.088388347648318440f, LOG2E = 1.4426950408889634f;
  float x[8];
  __builtin_amdgcn_sched_barrier(0);
#pragma unroll
  for (int i = 0; i < 4; ++i) {
    float d0 = 0.f, d1 = 0.f;
#pragma unroll
    for (int k = 0; k < 4; ++k) { d0 = dot2(X[i][k], q0[k], d0); d1 = dot2(X[i][k], q1[k], d1); }
    x[2 * i] = d0; x[2 * i + 1] = d1;
  }
  if (RELOAD) {
    PIN8(x);
    pace(t0, slot);
#pragma unroll
    for (int i = 0; i < 4; ++i) X[i] = __builtin_amdgcn_raw_buffer_load_b128(rx, vo + i * 1024, off, AUX_NT);
  }
  MEM_FENCE();
  __builtin_amdgcn_sched_barrier(0);
#define TR_STEP(HALF, CTRL, BIT) _Pragma("unroll") for (int h = 0; h < (HALF); ++h) { \
    const float lo_ = x[h], hi_ = x[h + (HALF)]; const float t1_ = lo_ + dpp<CTRL>(lo_), t2_ = hi_ + dpp<CTRL>(hi_); \
    x[h] = (c & (BIT)) ? t2_ : t1_; }
  TR_STEP(4, XOR7, 4) TR_STEP(2, XOR2, 2) TR_STEP(1, XOR1, 1)
#undef TR_STEP
  x[0] += dpp<XOR8>(x[0]);
  const float t = rbf(x[0]) * SCALE;
  const unsigned sb = cvtpk(t, t);
  *sc_slot = (unsigned short)sb;
  st.m = fmaxf(st.m, bflo(sb) * LOG2E);
}
template <bool RELOAD>
__device__ __forceinline__ void step_v(u32x4 (&X)[4], StreamState& st, const unsigned short* sc_slot,
                                       __amdgpu_buffer_rsrc_t rx, unsigned vo, unsigned off, unsigned t0, unsigned slot) {
  constexpr float LOG2E = 1.4426950408889634f;
  __builtin_amdgcn_sched_barrier(0);
  const float s = bf2f(*sc_slot) * LOG2E;
  const float fl = __builtin_floorf(s);
  const float p = __builtin_amdgcn_exp2f(s - fl) * __builtin_amdgcn_exp2f(fl - st.m);
  st.l += p;
  const unsigned w1 = cvtpk(p, dpp<ROR(14)>(p));
  u32x2 pb;
  pb[0] = w1; pb[1] = dppu<ROR(12)>(w1);
  u32x2 alo[4], ahi[4];
#pragma unroll
  for (int k = 0; k < 4; ++k)
#pragma unroll
    for (int j = 0; j < 2; ++j) {
      alo[k][j] = __builtin_amdgcn_perm(X[2 * j + 1][k], X[2 * j][k], 0x05040100u);
      ahi[k][j] = __builtin_amdgcn_perm(X[2 * j + 1][k], X[2 * j][k], 0x07060302u);
    }
  if (RELOAD) {
    asm volatile("" : "+v"(alo[0]), "+v"(alo[1]), "+v"(alo[2]), "+v"(alo[3]), "+v"(ahi[0]), "+v"(ahi[1]), "+v"(ahi[2]), "+v"(ahi[3]) :: "memory");
    pace(t0, slot);
#pragma unroll
    for (int i = 0; i < 4; ++i) X[i] = __builtin_amdgcn_raw_buffer_load_b128(rx, vo + i * 1024, off, AUX_NT);
  }
  MEM_FENCE();
  __builtin_amdgcn_sched_barrier(0);
#pragma unroll
  for (int k = 0; k < 4; ++k) {
    st.acc[2 * k] = __builtin_amdgcn_mfma_f32_16x16x16bf16_1k(__builtin_bit_cast(bf16x4, alo[k]), __builtin_bit_cast(bf16x4, pb), st.acc[2 * k], 0, 0, 0);
    st.acc[2 * k + 1] = __builtin_amdgcn_mfma_f32_16x16x16bf16_1k(__builtin_bit_cast(bf16x4, ahi[k]), __builtin_bit_cast(bf16x4, pb), st.acc[2 * k + 1], 0, 0, 0);
  }
}

struct ProArgs {
  __amdgpu_buffer_rsrc_t rwo, rwd, rgu, rk, rv;
  unsigned wo_base, wd_base, gu_base, so, so2;
  int gu_row;
};
struct Slot { int kind, idx; };
template <int N0, int N1, int N2> constexpr Slot slot_of(int s) {
  int c[3] = {0, 0, 0}; const int n[3] = {N0, N1, N2};
  int k = 0;
  for (int i = 0;; ++i) {
    while (c[k] >= n[k]) k = (k + 1) % 3;
    if (i == s) return Slot{k, c[k]};
    ++c[k]; k = (k + 1) % 3;
  }
}
template <bool J1, bool J3>
__device__ __forceinline__ void prologue(const ProArgs& a, float* lds, int wave, int lane, u32x4 (&K1)[4], u32x4 (&V1)[4]) {
  constexpr int N0 = 5, N1 = J3 ? 18 : 0, N2 = J1 ? NL1 : 0, N = N0 + N1 + N2;
  const unsigned vo = (unsigned)lane * 16u;
  const unsigned gvo = (unsigned)((lane / LPS) * F + (lane % LPS) * 8) * 2u;
  u32x4 ring[PWIN];
  auto issue = [&](auto sc) {
    constexpr int s = decltype(sc)::value;
    if constexpr (s < N) {
      constexpr Slot sl = slot_of<N0, N1, N2>(s);
      if constexpr (sl.kind == 0) {
        const int pc = min(wave + 4 * sl.idx, 17) * 64 + lane;
        ring[s % PWIN] = __builtin_amdgcn_raw_buffer_load_b128(a.rwo, (unsigned)((pc / 36) * D + (pc % 36) * 8) * 2u, a.wo_base, AUX_NT);
      } else if constexpr (sl.kind == 1) {
        ring[s % PWIN] = __builtin_amdgcn_raw_buffer_load_b128(a.rwd, vo, a.wd_base + (unsigned)sl.idx * 1024u, AUX_NT);
      } else {
        ring[s % PWIN] = __builtin_amdgcn_raw_buffer_load_b128(a.rgu, gvo, a.gu_base + (unsigned)sl.idx * (RPI * F * 2), AUX_NT);
      }
    } else if constexpr (s >= N + PWIN - 8 && s < N + PWIN - 4) {
      K1[s - (N + PWIN - 8)] = __builtin_amdgcn_raw_buffer_load_b128(a.rk, vo + (s - (N + PWIN - 8)) * 1024, a.so, AUX_NT);
    } else if constexpr (s >= N + PWIN - 4) {
      V1[s - (N + PWIN - 4)] = __builtin_amdgcn_raw_buffer_load_b128(a.rk, vo + (s - (N + PWIN - 4)) * 1024, a.so2, AUX_NT);
    }
  };
  float xv[NL1], acc[8] = {0, 0, 0, 0, 0, 0, 0, 0};
  auto consume = [&](auto sc) {
    constexpr int s = decltype(sc)::value;
    constexpr Slot sl = slot_of<N0, N1, N2>(s);
    if constexpr (sl.kind == 0) {
      *reinterpret_cast<u32x4*>(lds + L_WOR + (min(wave + 4 * sl.idx, 17) * 64 + lane) * 4) = ring[s % PWIN];
      MEM_FENCE();
    } else if constexpr (sl.kind == 1) {
      *reinterpret_cast<u32x4*>(lds + L_WD + ((wave * 18 + sl.idx) * 64 + lane) * 4) = ring[s % PWIN];
      MEM_FENCE();
    } else {
      if constexpr (sl.idx == 0) {
        lds_wait_ge(lds, L_SYNC + 4, 4u);
#pragma unroll
        for (int i = 0; i < NL1; ++i) xv[i] = lds[L_XN + a.gu_row + RPI * i + lane / LPS];
      }
      FMA8(acc, xv[sl.idx], ring[s % PWIN]);
      PIN8(acc);
    }
  };
  static_for<0, PWIN>([&](auto sc) { issue(sc); });
  MEM_FENCE();
  static_for<0, N>([&](auto sc) { consume(sc); issue(std::integral_constant<int, decltype(sc)::value + PWIN>{}); });
  if (J1) {
    *reinterpret_cast<f32x4*>(lds + L_GU + wave * 512 + lane * 8) = f32x4{acc[0], acc[1], acc[2], acc[3]};
    *reinterpret_cast<f32x4*>(lds + L_GU + wave * 512 + lane * 8 + 4) = f32x4{acc[4], acc[5], acc[6], acc[7]};
  }
  lds_signal(lds, L_SYNC + 5, lane);
}

__global__ void __launch_bounds__(512, 2) fused_kernel(Params p) {
  __shared__ __attribute__((aligned(16))) float lds[L_TOTAL];
  asm volatile("" :: "s"(p.x), "s"(p.kc), "s"(p.vc), "s"(p.wq), "s"(p.wo), "s"(p.wg), "s"(p.wu), "s"(p.wd), "s"(p.out), "s"(p.ws));
  const int tid = threadIdx.x, lane = tid & 63, wave = __builtin_amdgcn_readfirstlane(tid >> 6);
  const int b = blockIdx.x, kvh = b >> 4, split = b & 15;
  unsigned char* ws = p.ws;
  const __amdgpu_buffer_rsrc_t rs = __builtin_amdgcn_make_buffer_rsrc(ws, 0, (int)WS_END, 0x00020000);
  const int tt = ((b >> 3) + (b & 7) * 5) & 31;
  const bool has_j1 = tt < T1, has_j3 = tt >= 23, is_final = tt >= 14 && tt < 23;
  const int j1 = (b & 7) * T1 + tt;
  const int u = (b & 7) * 9 + tt - 23;
  const int fin = (b & 7) * 9 + tt - 14;
  const int rg = split >> 1, ch = split & 1;

  if (wave < 4) {
    const int r = lane >> 4, c = lane & 15;
    unsigned t0 = (unsigned)__builtin_amdgcn_s_memrealtime();
    constexpr unsigned TB = TILE * DH * 2, STEP = 8 * TB;
    const unsigned vo = (unsigned)lane * 16u;
    const unsigned so0 = (unsigned)((split & 7) * 512 + (split >> 3) * 4 + wave) * TB;
    ProArgs a;
    a.rk = __builtin_amdgcn_make_buffer_rsrc((void*)(p.kc + (size_t)kvh * SK * DH), 0, SK * DH * 2, 0x00020000);
    a.rv = __builtin_amdgcn_make_buffer_rsrc((void*)(p.vc + (size_t)kvh * SK * DH), 0, SK * DH * 2, 0x00020000);
    a.rwo = __builtin_amdgcn_make_buffer_rsrc((void*)p.wo, 0, 4096 * D * 2, 0x00020000);
    a.rwd = __builtin_amdgcn_make_buffer_rsrc((void*)p.wd, 0, F * D * 2, 0x00020000);
    a.rgu = __builtin_amdgcn_make_buffer_rsrc((void*)(j1 >= NCB * NRG ? p.wu : p.wg), 0, D * F * 2, 0x00020000);
    a.wo_base = (unsigned)((kvh * 256 + rg * 32) * D + ch * 288) * 2u;
    a.wd_base = (unsigned)(u * 64 * D) * 2u + (unsigned)wave * 18432u;
    a.gu_row = ((j1 % (NCB * NRG)) / NCB) * (576 / NRG) + wave * (144 / NRG);
    a.gu_base = (unsigned)(a.gu_row * F + (j1 % NCB) * GCOLS) * 2u;
    a.so = so0 + 2 * STEP; a.so2 = so0 + 3 * STEP;
    const __amdgpu_buffer_rsrc_t rk = a.rk, rv = a.rv;
    u32x4 X[NPK][4];
#pragma unroll
    for (int t = 0; t < 2; ++t)
#pragma unroll
      for (int i = 0; i < 4; ++i) X[t][i] = __builtin_amdgcn_raw_buffer_load_b128(rk, vo + i * 1024, so0 + t * STEP, AUX_NT);
    MEM_FENCE();
    __builtin_amdgcn_sched_barrier(0);
    __syncthreads();
    __builtin_amdgcn_sched_barrier(0);
    asm volatile("s_waitcnt vmcnt(4)" ::: "memory");
    if (has_j1) prologue<true, false>(a, lds, wave, lane, X[2], X[3]);
    else if (has_j3) prologue<false, true>(a, lds, wave, lane, X[2], X[3]);
    else prologue<false, false>(a, lds, wave, lane, X[2], X[3]);
#pragma unroll
    for (int t = 4; t < NPK; ++t) {
      asm volatile("s_waitcnt vmcnt(4)" ::: "memory");
      pace(t0, t + WSL - 2);
#pragma unroll
      for (int i = 0; i < 4; ++i) X[t][i] = __builtin_amdgcn_raw_buffer_load_b128(rk, vo + i * 1024, so0 + t * STEP, AUX_NT);
    }
    MEM_FENCE();
    __builtin_amdgcn_sched_barrier(0);
    lds_wait_ge(lds, L_SYNC + 3, 2u);
    t0 = __builtin_amdgcn_readfirstlane(__float_as_uint(lds[L_SYNC + 8]));
    const u32x4 q0 = *reinterpret_cast<const u32x4*>(reinterpret_cast<const unsigned short*>(lds + L_Q) + c * 8);
    const u32x4 q1 = *reinterpret_cast<const u32x4*>(reinterpret_cast<const unsigned short*>(lds + L_Q) + DH + c * 8);
    StreamState st;
    st.m = -INFINITY; st.l = 0.f;
#pragma unroll
    for (int e = 0; e < 8; ++e) st.acc[e] = f32x4{0.f, 0.f, 0.f, 0.f};
    unsigned short* sc = reinterpret_cast<unsigned short*>(lds + L_SC) + wave * 2048 + r * 8 + (c & 7);

    constexpr unsigned S0 = WSL - 2;
    step_k<true>(X[0], q0, q1, st, sc, rk, vo, so0 + NPK * STEP, c, t0, NPK + S0);
    step_k<true>(X[1], q0, q1, st, sc + 32, rk, vo, so0 + (NPK + 1) * STEP, c, t0, NPK + 1 + S0);
#pragma unroll
    for (int t = 2; t < NPK; ++t) step_k<false>(X[t], q0, q1, st, sc + 32 * t, rk, vo, 0u, c, t0, 0u);
    for (unsigned nt = NPK; nt + 2 < 64; nt += 2) {
      step_k<true>(X[0], q0, q1, st, sc + 32 * nt, rk, vo, so0 + (nt + 2) * STEP, c, t0, nt + 2 + S0);
      step_k<true>(X[1], q0, q1, st, sc + 32 * nt + 32, rk, vo, so0 + (nt + 3) * STEP, c, t0, nt + 3 + S0);
    }
    step_k<true>(X[0], q0, q1, st, sc + 32 * 62, rv, vo, so0, c, t0, 64 + S0);
    step_k<true>(X[1], q0, q1, st, sc + 32 * 63, rv, vo, so0 + STEP, c, t0, 65 + S0);
    {
      float pm = st.m; pm = fmaxf(pm, dpp<ROR(2)>(pm)); pm = fmaxf(pm, dpp<ROR(4)>(pm)); pm = xrow_max(pm);
      st.m = __builtin_ceilf(pm);
    }
    for (unsigned j = 0; j + 2 < 64; j += 2) {
      step_v<true>(X[0], st, sc + 32 * j, rv, vo, so0 + (j + 2) * STEP, t0, 66 + j + S0);
      step_v<true>(X[1], st, sc + 32 * j + 32, rv, vo, so0 + (j + 3) * STEP, t0, 67 + j + S0);
    }
    step_v<false>(X[0], st, sc + 32 * 62, rv, vo, 0u, t0, 0u);
    step_v<false>(X[1], st, sc + 32 * 63, rv, vo, 0u, t0, 0u);
    float l = st.l;
    l += dpp<XOR2>(l); l += dpp<ROR(4)>(l); l = xrow_sum(l);
    float* mrg = lds + L_MRG;
    if (c < 2) {
      float* rec = mrg + (wave * 2 + c) * 132;
#pragma unroll
      for (int i = 0; i < 4; ++i) {
        *reinterpret_cast<f32x4*>(rec + 4 + 8 * (4 * r + i)) = f32x4{st.acc[0][i], st.acc[1][i], st.acc[2][i], st.acc[3][i]};
        *reinterpret_cast<f32x4*>(rec + 4 + 8 * (4 * r + i) + 4) = f32x4{st.acc[4][i], st.acc[5][i], st.acc[6][i], st.acc[7][i]};
      }
      if (r == 0) { rec[0] = st.m; rec[1] = l; }
    }
    __syncthreads();
    if (wave < 2) {
      const int g = wave;
      Tag tag = {__float_as_uint(lds[L_SYNC]), __float_as_uint(lds[L_SYNC + 1])};
      float M = -INFINITY;
#pragma unroll
      for (int w = 0; w < 4; ++w) M = fmaxf(M, mrg[(w * 2 + g) * 132]);
      {
        const unsigned mb = __float_as_uint(M);
        M = __uint_as_float(((mb >> 31) ? mb : mb + 15u) & ~15u);
      }
      float L = 0.f, o0 = 0.f, o1 = 0.f;
#pragma unroll
      for (int w = 0; w < 4; ++w) {
        const float* rec = mrg + (w * 2 + g) * 132;
        const float wt = __builtin_amdgcn_exp2f(rec[0] - M);
        L = fmaf(wt, rec[1], L); o0 = fmaf(wt, rec[4 + 2 * lane], o0); o1 = fmaf(wt, rec[5 + 2 * lane], o1);
      }
      st2(rs, (unsigned)WS_PG + (unsigned)((b * 2 + g) * 144 + 2 * lane) * 8u, o0, o1, tag);
      if (lane < 8) st2(rs, (unsigned)WS_PG + (unsigned)((b * 2 + g) * 144 + 128 + 2 * lane) * 8u, M, L, tag);
    }
    return;
  }

  const int sv = wave - 4;
  const unsigned tsv = (unsigned)__builtin_amdgcn_s_memrealtime();
  __builtin_amdgcn_s_setprio(3);
  const int cb = b >> 2, rq = b & 3;
  const u64 epoch = __hip_atomic_load((GAS u64*)(ws + WS_EPOCH), __ATOMIC_RELAXED, __HIP_MEMORY_SCOPE_AGENT);
  unsigned xw[5];
#pragma unroll
  for (int i = 0; i < 5; ++i) xw[i] = reinterpret_cast<const unsigned*>(p.x)[(i < 4 ? lane : (lane & 31)) + 64 * i];
  u32x4 wqv[5];
  unsigned short xr[5];
  const int row0 = rq * 144 + sv * 36 + (lane >> 3);
#pragma unroll
  for (int i = 0; i < 5; ++i) {
    const int row = row0 + 8 * i - ((i == 4 && lane >= 32) ? 4 : 0);
    xr[i] = p.x[row];
  }
#pragma unroll
  for (int i = 0; i < 5; ++i) {
    const int row = row0 + 8 * i - ((i == 4 && lane >= 32) ? 4 : 0);
    wqv[i] = ldnt(p.wq + (size_t)row * 4096 + cb * 64 + (lane & 7) * 8);
  }
  if (tid >= 256 && tid < 256 + 6) lds[L_SYNC + 2 + (tid - 256)] = 0.f;
  __builtin_amdgcn_sched_barrier(0);
  __syncthreads();
  __builtin_amdgcn_sched_barrier(0);
  float m_b, den, rden;
  {
    const unsigned x4 = lane < 32 ? xw[4] : 0u;
    float s = bflo(x4) + bfhi(x4);
#pragma unroll
    for (int i = 0; i < 4; ++i) s += bflo(xw[i]) + bfhi(xw[i]);
    s = wave_sum(s);
    const float mean = s / (float)D;
    float ss = 0.f;
#pragma unroll
    for (int i = 0; i < 5; ++i) {
      const float d0 = bflo(xw[i]) - mean, d1 = bfhi(xw[i]) - mean;
      ss += (i < 4 || lane < 32) ? d0 * d0 + d1 * d1 : 0.f;
    }
    ss = wave_sum(ss);
    m_b = rbf(mean);
    den = rbf(sqrtf(rbf(rbf(ss / (float)D) + 1e-5f)));
    rden = __builtin_amdgcn_rcpf(den);
  }
  auto norm1 = [&](float xv) {
    const float a = rbf(xv - m_b), q = a * rden;
    return rbf(fmaf(fmaf(-q, den, a), rden, q));
  };
  {
    const unsigned xs = sv == 0 ? xw[0] : sv == 1 ? xw[1] : sv == 2 ? xw[2] : xw[3];
    *reinterpret_cast<float2*>(lds + L_XN + 2 * (lane + 64 * sv)) = make_float2(norm1(bflo(xs)), norm1(bfhi(xs)));
    if (sv == 0) *reinterpret_cast<float2*>(lds + L_XN + 2 * ((lane & 31) + 256)) = make_float2(norm1(bflo(xw[4])), norm1(bfhi(xw[4])));
    lds_signal(lds, L_SYNC + 4, lane);
  }
  Tag tag;
  { const u64 t = (epoch + 1ull) & ((1ull << 36) - 1ull); tag.lo = (unsigned)(t & 15ull) << 28; tag.hi = (unsigned)(t >> 4); }
  {
    float a[8] = {0, 0, 0, 0, 0, 0, 0, 0};
#pragma unroll
    for (int i = 0; i < 5; ++i) {
      const float xv = (i < 4 || lane < 32) ? norm1(bf2f(xr[i])) : 0.f;
      FMA8(a, xv, wqv[i]);
    }
    lds[L_QP + sv * 64 + (lane & 7) * 8 + (lane >> 3)] = treduce8(a, lane);
  }
  if (b == 0 && sv == 0 && lane < 8) {
    const u32x4 x = {(tsv & 0x0FFFFFFFu) | tag.lo, tag.hi, (tsv & 0x0FFFFFFFu) | tag.lo, tag.hi};
    __builtin_amdgcn_raw_buffer_store_b128(x, rs, (unsigned)WS_T0 + 16u * lane, 0, AUX_SC1);
  }
  if (tid == 256) { lds[L_SYNC] = __uint_as_float(tag.lo); lds[L_SYNC + 1] = __uint_as_float(tag.hi); }
  asm volatile("s_waitcnt vmcnt(0) lgkmcnt(0)" ::: "memory");
  unsigned arr = 0;
  if (lane == 0) arr = __hip_atomic_fetch_add((LAS unsigned*)(lds + L_SYNC + 2), 1u, __ATOMIC_RELAXED, __HIP_MEMORY_SCOPE_WORKGROUP);
  arr = __builtin_amdgcn_readfirstlane(arr);
  MEM_FENCE();
  if (arr == 3u) {
    const float s = (lds[L_QP + lane] + lds[L_QP + 64 + lane]) + (lds[L_QP + 128 + lane] + lds[L_QP + 192 + lane]);
    st1(rs, (unsigned)WS_QG + (unsigned)(b * 64 + lane) * 8u, s, tag);
  }

  __builtin_amdgcn_s_setprio(0);
  if (sv == 0) {
    lds_wait_ge(lds, L_SYNC + 5, 4u);
#pragma unroll
    for (int rd = 0; rd < 3; ++rd) {
      const int blk = min(rd * 64 + lane, 143), h = blk / 36, cg = blk % 36;
      u32x4 w[8];
#pragma unroll
      for (int i = 0; i < 8; ++i) w[i] = *reinterpret_cast<const u32x4*>(lds + L_WOR + ((8 * h + i) * 36 + cg) * 4);
      u32x4* dst = reinterpret_cast<u32x4*>(lds + L_WO) + (cg >> 1) * 64 + h * 16 + (cg & 1) * 8;
#pragma unroll
      for (int k = 0; k < 4; ++k) {
        u32x4 lo, hi;
#pragma unroll
        for (int j = 0; j < 4; ++j) {
          lo[j] = __builtin_amdgcn_perm(w[2 * j + 1][k], w[2 * j][k], 0x05040100u);
          hi[j] = __builtin_amdgcn_perm(w[2 * j + 1][k], w[2 * j][k], 0x07060302u);
        }
        dst[2 * k] = lo; dst[2 * k + 1] = hi;
      }
    }
  } else if (sv == 1) {
    lds_wait_ge(lds, L_SYNC + 5, 4u);
    if (has_j1) {
      float s0 = 0.f, s1 = 0.f;
#pragma unroll
      for (int i = 0; i < 4 * RPI; ++i) { const float2 t = *reinterpret_cast<const float2*>(lds + L_GU + i * GCOLS + 2 * lane); s0 += t.x; s1 += t.y; }
      st2(rs, (unsigned)WS_AG + (unsigned)((j1 / NCB) * F + (j1 % NCB) * GCOLS + 2 * lane) * 8u, s0, s1, tag);
    }
    if (has_j3) {
      for (int i = 0; i < 2; ++i) __builtin_amdgcn_s_sleep(127);
      float t0 = 0.f, t1 = 0.f;
      {
        const unsigned off = (unsigned)WS_AG + (unsigned)((lane >> 5) * NRG * F + u * 64 + (lane & 31) * 2) * 8u;
        u32x4 gn[NRG];
        gather<NRG, F * 8, 64>(rs, off, off + (unsigned)(NRG - 1) * (F * 8u), tag, gn, SPIN_CAP >> 5);
#pragma unroll
        for (int k = 0; k < NRG; ++k) { t0 += g_dec(gn[k][0]); t1 += g_dec(gn[k][2]); }
        t0 = rbf(t0); t1 = rbf(t1);
      }
      const auto x0 = __builtin_amdgcn_permlane32_swap(__float_as_uint(t0), __float_as_uint(t0), false, false);
      const auto x1 = __builtin_amdgcn_permlane32_swap(__float_as_uint(t1), __float_as_uint(t1), false, false);
      const float h0 = rbf(rbf(silu_f(__uint_as_float(x0[0]))) * __uint_as_float(x0[1])), h1 = rbf(rbf(silu_f(__uint_as_float(x1[0]))) * __uint_as_float(x1[1]));
      if (lane < 32) *reinterpret_cast<float2*>(lds + L_Y + 576 + 2 * lane) = make_float2(h0, h1);
      float y[8] = {0, 0, 0, 0, 0, 0, 0, 0}, z[8] = {0, 0, 0, 0, 0, 0, 0, 0};
#pragma unroll 8
      for (int rr = 0; rr < 64; ++rr) {
        const u32x4 w = *reinterpret_cast<const u32x4*>(lds + L_WD + rr * 288 + lane * 4);
        const float hv = lds[L_Y + 576 + rr];
        FMA8(y, hv, w);
      }
#pragma unroll
      for (int o = 0; o < 8; ++o) {
        const u32x4 w = *reinterpret_cast<const u32x4*>(lds + L_WD + (o * 8 + (lane >> 3)) * 288 + 256 + (lane & 7) * 4);
        const float hv = lds[L_Y + 576 + o * 8 + (lane >> 3)];
        FMA8(z, hv, w);
      }
      const float zt = treduce8(z, lane);
      *reinterpret_cast<f32x4*>(lds + L_Y + lane * 8) = f32x4{y[0], y[1], y[2], y[3]};
      *reinterpret_cast<f32x4*>(lds + L_Y + lane * 8 + 4) = f32x4{y[4], y[5], y[6], y[7]};
      lds[L_Y + 512 + (lane & 7) * 8 + (lane >> 3)] = zt;
    }
  } else {
    {
      const unsigned off = (unsigned)WS_QG + (unsigned)((kvh * 16 + (sv - 2) * 8 + (lane >> 5) * 4) * 64 + (lane & 31) * 2) * 8u;
      u32x4 g4[5];
      __builtin_amdgcn_s_sleep(127);
      gather<5, 512, 16>(rs, off, (unsigned)WS_T0, tag, g4, SPIN_CAP);
      if (sv == 2 && lane == 0) { const int d = (int)((tsv - g4[4][0]) << 4) >> 4; lds[L_SYNC + 8] = __uint_as_float(tsv - (unsigned)d); }
      const float qa = (g_dec(g4[0][0]) + g_dec(g4[1][0])) + (g_dec(g4[2][0]) + g_dec(g4[3][0]));
      const float qb = (g_dec(g4[0][2]) + g_dec(g4[1][2])) + (g_dec(g4[2][2]) + g_dec(g4[3][2]));
      reinterpret_cast<unsigned*>(lds + L_Q)[(sv - 2) * 64 + lane] = cvtpk(qa, qb);
      lds_signal(lds, L_SYNC + 3, lane);
    }
  }
  __builtin_amdgcn_s_setprio(3);
  __syncthreads();
  if (sv == 0) {
    const int R = lane >> 4, c = lane & 15;
    u32x4 bfr[18];
#pragma unroll
    for (int j = 0; j < 18; ++j) bfr[j] = *reinterpret_cast<const u32x4*>(lds + L_WO + (j * 64 + lane) * 4);
    const unsigned rec = (unsigned)WS_PG + (unsigned)(((kvh * 16 + c) * 2 + (rg >> 2)) * 144) * 8u;
    const unsigned so0 = (unsigned)WS_SG + (unsigned)((kvh * 8 + rg) * 576 + ch * 288 + lane) * 8u;
    u32x4 g5[5];
    __builtin_amdgcn_s_sleep(8);
    gather<5, 16, 1>(rs, rec + (unsigned)((rg & 3) * 32 + R * 8) * 8u, rec + 128u * 8u, tag, g5, SPIN_CAP);
    const float ms = g_dec(g5[4][0]), ls = g_dec(g5[4][2]);
    const float M = row_max(ms);
    const float w = __builtin_amdgcn_exp2f(ms - M);
    const float L = row_sum(w * ls);
    float inv = __builtin_amdgcn_rcpf(L);
    inv = fmaf(fmaf(-L, inv, 1.f), inv, inv);
    u32x4 a;
#pragma unroll
    for (int k = 0; k < 4; ++k) {
      const float o0 = row_sum(w * g_dec(g5[k][0])) * inv, o1 = row_sum(w * g_dec(g5[k][2])) * inv;
      a[k] = cvtpk(o0, o1);
    }
    auto mm = [&](const u32x4& bf) -> float {
      const f32x4 d = __builtin_amdgcn_mfma_f32_16x16x32_bf16(__builtin_bit_cast(bf16x8, a), __builtin_bit_cast(bf16x8, bf), f32x4{0.f, 0.f, 0.f, 0.f}, 0, 0, 0);
      return d[0];
    };
    const bool r1 = (R & 1) != 0, r2 = (R & 2) != 0;
#pragma unroll
    for (int k = 0; k < 4; ++k) {
      const float e0 = mm(bfr[4 * k]), e1 = mm(bfr[4 * k + 1]), e2 = mm(bfr[4 * k + 2]), e3 = mm(bfr[4 * k + 3]);
      st1(rs, so0 + (unsigned)k * 512u, r2 ? (r1 ? e3 : e2) : (r1 ? e1 : e0), tag);
    }
    {
      const float e0 = mm(bfr[16]), e1 = mm(bfr[17]);
      if (R < 2) st1(rs, so0 + 2048u, r1 ? e1 : e0, tag);
    }
    lds_signal(lds, L_SYNC + 6, lane);
  } else if (sv == 1) {
    if (has_j3) {
#pragma unroll
      for (int k = 0; k < 5; ++k) {
        const int col = 128 * k + 2 * lane;
        if (k < 4 || lane < 32) st2(rs, (unsigned)WS_YG + (unsigned)(u * 576 + col) * 8u, lds[L_Y + col], lds[L_Y + col + 1], tag);
      }
    }
  } else if (sv == 3) {
    if (is_final) {
      const unsigned off = (unsigned)WS_YG + (unsigned)((lane >> 2) * 576 + fin * 8 + (lane & 3) * 2) * 8u;
      u32x4 g5[5];
      __builtin_amdgcn_s_sleep(32);
      gather<5, 16 * 576 * 8, 12>(rs, off, off + (unsigned)(lane < 32 ? 64 : 56) * (576u * 8u), tag, g5, SPIN_CAP);
      float y0 = lane < 32 ? g_dec(g5[4][0]) : 0.f, y1 = lane < 32 ? g_dec(g5[4][2]) : 0.f;
#pragma unroll
      for (int k = 0; k < 4; ++k) { y0 += g_dec(g5[k][0]); y1 += g_dec(g5[k][2]); }
      y0 += dpp<ROR(4)>(y0); y0 += dpp<ROR(8)>(y0); y0 = xrow_sum(y0);
      y1 += dpp<ROR(4)>(y1); y1 += dpp<ROR(8)>(y1); y1 = xrow_sum(y1);
      if (lane < 4) *reinterpret_cast<float2*>(lds + L_QP + 2 * lane) = make_float2(rbf(silu_f(rbf(y0))), rbf(silu_f(rbf(y1))));
      lds_signal(lds, L_SYNC + 7, lane);
    }
  } else if (sv == 2 && is_final) {
    const unsigned xab = reinterpret_cast<const unsigned*>(p.x)[fin * 4 + (lane & 3)];
    const unsigned off = (unsigned)WS_SG + (unsigned)((lane >> 2) * 576 + fin * 8 + (lane & 3) * 2) * 8u;
    u32x4 g8[8];
    lds_wait_ge(lds, L_SYNC + 6, 1u);
    __builtin_amdgcn_s_sleep(8);
    gather<8, 16 * 576 * 8, 1>(rs, off, off + 7u * (16u * 576u * 8u), tag, g8, SPIN_CAP);
    float s0 = 0.f, s1 = 0.f;
#pragma unroll
    for (int k = 0; k < 8; ++k) { s0 += g_dec(g8[k][0]); s1 += g_dec(g8[k][2]); }
    s0 += dpp<ROR(4)>(s0); s0 += dpp<ROR(8)>(s0); s0 = xrow_sum(s0);
    s1 += dpp<ROR(4)>(s1); s1 += dpp<ROR(8)>(s1); s1 = xrow_sum(s1);
    lds_wait_ge(lds, L_SYNC + 7, 1u);
    const float gate0 = lds[L_QP + 2 * (lane & 3)], gate1 = lds[L_QP + 2 * (lane & 3) + 1];
    const float o0 = bflo(xab) + rbf(rbf(s0) * gate0), o1 = bfhi(xab) + rbf(rbf(s1) * gate1);
    if (lane < 4) reinterpret_cast<unsigned*>(p.out)[fin * 4 + lane] = cvtpk(o0, o1);
    if (fin == 0 && lane == 0) __hip_atomic_store((GAS u64*)(ws + WS_EPOCH), epoch + 1ull, __ATOMIC_RELAXED, __HIP_MEMORY_SCOPE_AGENT);
  }
}

extern "C" void kernel_launch(void* const* d_in, const int* in_sizes, int n_in, void* d_out, int out_size, void* d_ws, size_t ws_size,
                              hipStream_t stream) {
  Params p;
  p.x = (const unsigned short*)d_in[0];
  p.kc = (const unsigned short*)d_in[1];
  p.vc = (const unsigned short*)d_in[2];
  p.wq = (const unsigned short*)d_in[3];
  p.wo = (const unsigned short*)d_in[4];
  p.wg = (const unsigned short*)d_in[6];
  p.wu = (const unsigned short*)d_in[7];
  p.wd = (const unsigned short*)d_in[8];
  p.out = (unsigned short*)d_out;
  p.ws = (unsigned char*)d_ws;
  fused_kernel<<<256, 512, 0, stream>>>(p);
}
```
